```python
import torch

def _softmax(x, axis=-1):
    return torch.softmax(x, dim=axis)

def _gelu(x):
    return torch.nn.functional.gelu(x, approximate='tanh')

def _silu(x):
    return torch.nn.functional.silu(x)

def _sigmoid(x):
    return torch.sigmoid(x)

def _layernorm(x, axis=-1, eps=1e-05):
    m = x.mean(dim=axis, keepdim=True)
    v = x.var(dim=axis, keepdim=True, unbiased=False)
    return (x - m) / torch.sqrt(v + eps)

def _scatter_add(tgt, idx, src, axis=0):
    return tgt.clone().index_add_(axis, idx, src)

def _segment_sum(src, seg, n):
    out = torch.zeros((n,) + src.shape[1:], dtype=src.dtype)
    return out.index_add_(0, seg, src)

def _pad(x, pw):
    flat = tuple((v for pair in reversed(pw) for v in pair))
    return torch.nn.functional.pad(x, flat)
kernel_name = 'adaLN_cond__gqa_block_cached__geglu_ffn__decode'

def setup_inputs():

    _w = lambda a: (lambda t: t.to(torch.bfloat16) if t.is_floating_point() else t)(torch.as_tensor(a))

    def _A_setup():
        B, Sq, D = (1, 1, 1024)
        Hq, Hkv, Sk, Dh = (32, 4, 65536, 128)
        assert Hq % Hkv == 0, (Hq, Hkv)
        return {'x': _w((lambda *s: torch.randn(*s, dtype=torch.bfloat16) * 0.05)(B, Sq, D)), 'k_cache': _w((lambda *s: torch.randn(*s, dtype=torch.bfloat16) * 3.2)(B, Hkv, Sk, Dh)), 'v_cache': _w((lambda *s: torch.randn(*s, dtype=torch.bfloat16) * 0.4)(B, Hkv, Sk, Dh)), 'wq': _w((lambda *s: torch.randn(*s, dtype=torch.bfloat16) * 3.2)(D, Hq * Dh) * D ** (-0.5)), 'wo': _w((lambda *s: torch.randn(*s, dtype=torch.bfloat16) * 0.4)(Hq * Dh, D) * (Hq * Dh) ** (-0.5))}
    _A_in = _A_setup()

    def _B_setup():
        B, S, D, F = (1, 1, 1024, 3584)
        return {'x': _w((lambda *s: torch.randn(*s, dtype=torch.bfloat16) * 0.05)(B, S, D)), 'w_gate': _w((lambda *s: torch.randn(*s, dtype=torch.bfloat16) * 0.05)(D, F)), 'w_up': _w((lambda *s: torch.randn(*s, dtype=torch.bfloat16) * 0.05)(D, F)), 'w_down': _w((lambda *s: torch.randn(*s, dtype=torch.bfloat16) * 0.05)(F, D))}
    _B_in = _B_setup()
    return {'A_x': _A_in['x'], 'A_k_cache': _A_in['k_cache'], 'A_v_cache': _A_in['v_cache'], 'A_wq': _A_in['wq'], 'A_wo': _A_in['wo'], 'B_x': _B_in['x'], 'B_w_gate': _B_in['w_gate'], 'B_w_up': _B_in['w_up'], 'B_w_down': _B_in['w_down'], 'c': _w((lambda *s: torch.randn(*s, dtype=torch.bfloat16) * 0.4)(1, 1024) * 8.0), 'w_mod': _w((lambda *s: torch.randn(*s, dtype=torch.bfloat16) * 0.4)(1024, 6 * 1024) * 1024 ** (-0.5) * 8.0)}

def reference(A_x, A_k_cache, A_v_cache, A_wq, A_wo, B_x, B_w_gate, B_w_up, B_w_down, c, w_mod):

    def _A_ref(x, k_cache, v_cache, wq, wo):
        B, Sq, D = x.shape
        Hq, Dh = (32, 128)
        Hkv = k_cache.shape[1]
        rep = Hq // Hkv
        q = (lambda x, *d: x.permute(*d))((x @ wq).reshape(B, Sq, Hq, Dh), 0, 2, 1, 3)
        k = (lambda x, n, axis: torch.repeat_interleave(x, n, dim=axis))(k_cache, rep, axis=1)
        v = (lambda x, n, axis: torch.repeat_interleave(x, n, dim=axis))(v_cache, rep, axis=1)
        scores = torch.einsum('bhqd,bhkd->bhqk', q, k) * Dh ** (-0.5)
        p = _softmax(scores, axis=-1)
        o = torch.einsum('bhqk,bhkd->bhqd', p, v)
        return (lambda x, *d: x.permute(*d))(o, 0, 2, 1, 3).reshape(B, Sq, Hq * Dh) @ wo

    def _B_ref(x, w_gate, w_up, w_down):
        return _gelu(x @ w_gate) * (x @ w_up) @ w_down
    x = A_x
    mod = (_silu(c) @ w_mod).reshape(c.shape[0], 6, 1, -1)
    sa, ca, ga, sb, cb, gb = (mod[:, 0].clone(), mod[:, 1].clone(), mod[:, 2].clone(), mod[:, 3].clone(), mod[:, 4].clone(), mod[:, 5].clone())
    h = _layernorm(x, axis=-1) * (1 + ca) + sa
    x = x + ga * _A_ref(h, A_k_cache, A_v_cache, A_wq, A_wo)
    h = _layernorm(x, axis=-1) * (1 + cb) + sb
    x = x + gb * _B_ref(h, B_w_gate, B_w_up, B_w_down)
    return x
```

```cpp
#include <hip/hip_runtime.h>
#include <hip/hip_bf16.h>
#include <stdint.h>

typedef unsigned short bf16s;
typedef unsigned u32x4 __attribute__((ext_vector_type(4)));
typedef float f32x4 __attribute__((ext_vector_type(4)));
typedef short bf16x8 __attribute__((ext_vector_type(8)));

constexpr int DM = 1024, HQ = 32, HKV = 4, SK = 65536, DH = 128, FF = 3584, NMOD = 6144, DQ = HQ * DH;
constexpr int NSPLIT = 64;
constexpr int SM = 8, SQ = 4, S1 = 16, S2 = 8, S3 = 8;

constexpr size_t WS_MODP = 0;
constexpr size_t WS_MODF = WS_MODP + (size_t)SM * NMOD;
constexpr size_t WS_QP   = WS_MODF + NMOD;
constexpr size_t WS_PML  = WS_QP + (size_t)SQ * DQ;
constexpr size_t WS_PO   = WS_PML + (size_t)HQ * NSPLIT * 2;
constexpr size_t WS_ATTP = WS_PO + (size_t)HQ * NSPLIT * DH;
constexpr size_t WS_X1   = WS_ATTP + (size_t)S1 * DM;
constexpr size_t WS_GUP  = WS_X1 + DM;
constexpr size_t WS_DNP  = WS_GUP + (size_t)S2 * 2 * FF;
constexpr size_t WS_END  = WS_DNP + (size_t)S3 * DM;

__device__ __forceinline__ float bf2f(bf16s u) { return __builtin_bit_cast(float, (unsigned)u << 16); }
__device__ __forceinline__ float rbf(float f) { return __bfloat162float(__float2bfloat16(f)); }
__device__ __forceinline__ unsigned cvtpk(float lo, float hi) {
  unsigned r; asm volatile("v_cvt_pk_bf16_f32 %0, %1, %2" : "=v"(r) : "v"(lo), "v"(hi)); return r;
}
__device__ __forceinline__ u32x4 ldnt(const void* p) { return __builtin_nontemporal_load(reinterpret_cast<const u32x4*>(p)); }
__device__ __forceinline__ float lo_f(unsigned u) { return __builtin_bit_cast(float, u << 16); }
__device__ __forceinline__ float hi_f(unsigned u) { return __builtin_bit_cast(float, u & 0xffff0000u); }

template <int R, int C, int NW>
struct GemvTile {
  static constexpr int LPR = C / 8, RPI = 64 / LPR, NI = R / (RPI * NW);
  static_assert(NI * RPI * NW == R, "tile");
  u32x4 w[NI];
  __device__ __forceinline__ void load(const bf16s* W, int ldw, int row0, int col0, int wave, int lane) {
#pragma unroll
    for (int i = 0; i < NI; ++i) {
      const int row = row0 + (i * NW + wave) * RPI + lane / LPR;
      w[i] = ldnt(W + (size_t)row * ldw + col0 + (lane % LPR) * 8);
    }
  }
  __device__ __forceinline__ void fma(const float* xs, int wave, int lane, float (&acc)[8]) {
#pragma unroll
    for (int i = 0; i < NI; ++i) {
      const float xv = xs[(i * NW + wave) * RPI + lane / LPR];
#pragma unroll
      for (int k = 0; k < 4; ++k) {
        acc[2 * k] = fmaf(xv, lo_f(w[i][k]), acc[2 * k]);
        acc[2 * k + 1] = fmaf(xv, hi_f(w[i][k]), acc[2 * k + 1]);
      }
    }
  }
  __device__ __forceinline__ static void wave_reduce_store(float (&acc)[8], float* red, int wave, int lane) {
#pragma unroll
    for (int off = LPR; off < 64; off <<= 1)
#pragma unroll
      for (int k = 0; k < 8; ++k) acc[k] += __shfl_xor(acc[k], off);
    if (lane < LPR) {
      *reinterpret_cast<f32x4*>(red + wave * C + lane * 8) = f32x4{acc[0], acc[1], acc[2], acc[3]};
      *reinterpret_cast<f32x4*>(red + wave * C + lane * 8 + 4) = f32x4{acc[4], acc[5], acc[6], acc[7]};
    }
  }
};

__device__ __forceinline__ float block_sum256(float v, float* sred, int tid) {
#pragma unroll
  for (int off = 32; off >= 1; off >>= 1) v += __shfl_xor(v, off);
  __syncthreads();
  if ((tid & 63) == 0) sred[tid >> 6] = v;
  __syncthreads();
  return (sred[0] + sred[1]) + (sred[2] + sred[3]);
}

__device__ __forceinline__ void ln_stats(const float (&xv)[4], float* sred, int tid, float& mb, float& den) {
  const float mean = block_sum256((xv[0] + xv[1]) + (xv[2] + xv[3]), sred, tid) * (1.0f / DM);
  float s = 0.f;
#pragma unroll
  for (int k = 0; k < 4; ++k) { const float d = xv[k] - mean; s = fmaf(d, d, s); }
  const float var = block_sum256(s, sred, tid) * (1.0f / DM);
  mb = rbf(mean);
  den = rbf(sqrtf(rbf(rbf(var) + 1e-5f)));
}
__device__ __forceinline__ float ada_h(float x, float mb, float den, float cmod, float smod) {
  const float xn = rbf(rbf(x - mb) / den);
  return rbf(rbf(xn * rbf(1.0f + cmod)) + smod);
}

__global__ void __launch_bounds__(256) k_mod(const bf16s* __restrict__ c, const bf16s* __restrict__ wmod, float* __restrict__ ws) {
  __shared__ __attribute__((aligned(16))) float xs[128];
  __shared__ __attribute__((aligned(16))) float red[4 * 64];
  const int tid = threadIdx.x, lane = tid & 63, wave = tid >> 6;
  const int cg = blockIdx.x % 96, ks = blockIdx.x / 96;
  GemvTile<128, 64, 4> T;
  T.load(wmod, NMOD, ks * 128, cg * 64, wave, lane);
  if (tid < 128) { const float cv = bf2f(c[ks * 128 + tid]); xs[tid] = rbf(cv / (1.0f + __expf(-cv))); }
  __syncthreads();
  float acc[8] = {0, 0, 0, 0, 0, 0, 0, 0};
  T.fma(xs, wave, lane, acc);
  T.wave_reduce_store(acc, red, wave, lane);
  __syncthreads();
  if (tid < 64) ws[WS_MODP + (size_t)ks * NMOD + cg * 64 + tid] = (red[tid] + red[64 + tid]) + (red[128 + tid] + red[192 + tid]);
}

__global__ void __launch_bounds__(256) k_q(const bf16s* __restrict__ x, const bf16s* __restrict__ wq, float* __restrict__ ws) {
  __shared__ __attribute__((aligned(16))) float xs[256];
  __shared__ __attribute__((aligned(16))) float red[4 * 64];
  __shared__ float sred[4];
  const int tid = threadIdx.x, lane = tid & 63, wave = tid >> 6;
  const int cg = blockIdx.x % 64, ks = blockIdx.x / 64;
  GemvTile<256, 64, 4> T;
  T.load(wq, DQ, ks * 256, cg * 64, wave, lane);
  float xv[4];
#pragma unroll
  for (int k = 0; k < 4; ++k) xv[k] = bf2f(x[tid * 4 + k]);
  const int r = ks * 256 + tid;
  float sa = 0.f, ca = 0.f;
#pragma unroll
  for (int s = 0; s < SM; ++s) { sa += ws[WS_MODP + (size_t)s * NMOD + r]; ca += ws[WS_MODP + (size_t)s * NMOD + DM + r]; }
  float mb, den;
  ln_stats(xv, sred, tid, mb, den);
  xs[tid] = ada_h(bf2f(x[r]), mb, den, rbf(ca), rbf(sa));
  __syncthreads();
  float acc[8] = {0, 0, 0, 0, 0, 0, 0, 0};
  T.fma(xs, wave, lane, acc);
  T.wave_reduce_store(acc, red, wave, lane);
  __syncthreads();
  if (tid < 64) ws[WS_QP + (size_t)ks * DQ + cg * 64 + tid] = (red[tid] + red[64 + tid]) + (red[128 + tid] + red[192 + tid]);
}

constexpr int REC = 4 + DH;
__global__ void __launch_bounds__(512, 2) k_attn(const bf16s* __restrict__ kc, const bf16s* __restrict__ vc, float* __restrict__ ws) {
  __shared__ __attribute__((aligned(16))) bf16s qs[8 * DH];
  __shared__ __attribute__((aligned(16))) float mrg[8 * 8 * REC];
  const int tid = threadIdx.x, lane = tid & 63, wave = __builtin_amdgcn_readfirstlane(tid >> 6);
  const int g = blockIdx.x >> 6, sp = blockIdx.x & 63;
  const int lg = lane >> 4, lc = lane & 15;
  const size_t key0 = (size_t)g * SK + (size_t)sp * 1024 + wave * 128;
  u32x4 K[8][4];
  const bf16s* kb = kc + (key0 + lc) * DH + lg * 8;
#pragma unroll
  for (int t = 0; t < 8; ++t)
#pragma unroll
    for (int ks = 0; ks < 4; ++ks) K[t][ks] = ldnt(kb + (size_t)t * 16 * DH + ks * 32);
  u32x4 V[4][8];
  const bf16s* vb = vc + (key0 + 4 * lg) * DH + lc * 8;
#pragma unroll
  for (int t = 0; t < 2; ++t)
#pragma unroll
    for (int i = 0; i < 8; ++i) V[t][i] = ldnt(vb + (size_t)(t * 32 + (i >> 2) * 16 + (i & 3)) * DH);
  {
    const int idx = blockIdx.x * 512 + tid;
    if (idx < NMOD) {
      float s = 0.f;
#pragma unroll
      for (int k = 0; k < SM; ++k) s += ws[WS_MODP + (size_t)k * NMOD + idx];
      ws[WS_MODF + idx] = rbf(s);
    }
  }
  for (int e = tid; e < 8 * DH; e += 512) {
    float s = 0.f;
#pragma unroll
    for (int k = 0; k < SQ; ++k) s += ws[WS_QP + (size_t)k * DQ + g * 8 * DH + e];
    qs[e] = __builtin_bit_cast(bf16s, __float2bfloat16(s));
  }
  __syncthreads();
  bf16x8 qf[4];
#pragma unroll
  for (int ks = 0; ks < 4; ++ks) {
    u32x4 z = {0u, 0u, 0u, 0u};
    if (lc < 8) z = *reinterpret_cast<const u32x4*>(qs + lc * DH + ks * 32 + lg * 8);
    qf[ks] = __builtin_bit_cast(bf16x8, z);
  }
  float sc[8][4];
#pragma unroll
  for (int t = 0; t < 8; ++t) {
    f32x4 d = {0.f, 0.f, 0.f, 0.f};
#pragma unroll
    for (int ks = 0; ks < 4; ++ks) d = __builtin_amdgcn_mfma_f32_16x16x32_bf16(__builtin_bit_cast(bf16x8, K[t][ks]), qf[ks], d, 0, 0, 0);
#pragma unroll
    for (int r = 0; r < 4; ++r) sc[t][r] = rbf(rbf(d[r]) * 0.08838834764831845f);
  }
#pragma unroll
  for (int t = 2; t < 4; ++t)
#pragma unroll
    for (int i = 0; i < 8; ++i) V[t][i] = ldnt(vb + (size_t)(t * 32 + (i >> 2) * 16 + (i & 3)) * DH);
  float m = sc[0][0];
#pragma unroll
  for (int t = 0; t < 8; ++t)
#pragma unroll
    for (int r = 0; r < 4; ++r) m = fmaxf(m, sc[t][r]);
  m = fmaxf(m, __shfl_xor(m, 16));
  m = fmaxf(m, __shfl_xor(m, 32));
  float l = 0.f;
  u32x4 P[4];
#pragma unroll
  for (int t = 0; t < 4; ++t) {
    float p[8];
#pragma unroll
    for (int j = 0; j < 8; ++j) { p[j] = __builtin_amdgcn_exp2f((sc[2 * t + (j >> 2)][j & 3] - m) * 1.4426950408889634f); l += p[j]; }
#pragma unroll
    for (int k = 0; k < 4; ++k) P[t][k] = cvtpk(p[2 * k], p[2 * k + 1]);
  }
  l += __shfl_xor(l, 16);
  l += __shfl_xor(l, 32);
  f32x4 acc[8];
#pragma unroll
  for (int d = 0; d < 8; ++d) acc[d] = f32x4{0.f, 0.f, 0.f, 0.f};
#pragma unroll
  for (int t = 0; t < 4; ++t) {
#pragma unroll
    for (int d = 0; d < 8; ++d) {
      u32x4 b;
#pragma unroll
      for (int p = 0; p < 4; ++p)
        b[p] = __builtin_amdgcn_perm(V[t][2 * p + 1][d >> 1], V[t][2 * p][d >> 1], (d & 1) ? 0x07060302u : 0x05040100u);
      acc[d] = __builtin_amdgcn_mfma_f32_16x16x32_bf16(__builtin_bit_cast(bf16x8, P[t]), __builtin_bit_cast(bf16x8, b), acc[d], 0, 0, 0);
    }
  }
  if (lg < 2) {
#pragma unroll
    for (int r = 0; r < 4; ++r) {
      float* rec = mrg + (wave * 8 + 4 * lg + r) * REC + 4 + 8 * lc;
      *reinterpret_cast<f32x4*>(rec) = f32x4{acc[0][r], acc[1][r], acc[2][r], acc[3][r]};
      *reinterpret_cast<f32x4*>(rec + 4) = f32x4{acc[4][r], acc[5][r], acc[6][r], acc[7][r]};
    }
  }
  if (lane < 8) { mrg[(wave * 8 + lane) * REC] = m; mrg[(wave * 8 + lane) * REC + 1] = l; }
  __syncthreads();
  {
    const int h = tid >> 6, d0 = (tid & 63) * 2;
    float M = mrg[h * REC];
#pragma unroll
    for (int w = 1; w < 8; ++w) M = fmaxf(M, mrg[(w * 8 + h) * REC]);
    float L = 0.f, o0 = 0.f, o1 = 0.f;
#pragma unroll
    for (int w = 0; w < 8; ++w) {
      const float* rec = mrg + (w * 8 + h) * REC;
      const float wt = __builtin_amdgcn_exp2f((rec[0] - M) * 1.4426950408889634f);
      L = fmaf(wt, rec[1], L); o0 = fmaf(wt, rec[4 + d0], o0); o1 = fmaf(wt, rec[5 + d0], o1);
    }
    const size_t slot = (size_t)(g * 8 + h) * NSPLIT + sp;
    *reinterpret_cast<float2*>(ws + WS_PO + slot * DH + d0) = make_float2(o0, o1);
    if ((tid & 63) == 0) *reinterpret_cast<float2*>(ws + WS_PML + slot * 2) = make_float2(M, L);
  }
}

__global__ void __launch_bounds__(256) k_wo(const bf16s* __restrict__ wo, float* __restrict__ ws) {
  __shared__ __attribute__((aligned(16))) float xs[256];
  __shared__ __attribute__((aligned(16))) float red[4 * 64];
  __shared__ __attribute__((aligned(16))) float wts[2 * NSPLIT];
  __shared__ __attribute__((aligned(16))) float part[4 * 256];
  __shared__ float linv[2];
  const int tid = threadIdx.x, lane = tid & 63, wave = tid >> 6;
  const int cg = blockIdx.x % 16, hp = blockIdx.x / 16;
  GemvTile<256, 64, 4> T;
  T.load(wo, DM, hp * 256, cg * 64, wave, lane);
  if (wave < 2) {
    const float2 ml = *reinterpret_cast<const float2*>(ws + WS_PML + ((size_t)(hp * 2 + wave) * NSPLIT + lane) * 2);
    float M = ml.x;
#pragma unroll
    for (int off = 32; off >= 1; off >>= 1) M = fmaxf(M, __shfl_xor(M, off));
    const float wt = __builtin_amdgcn_exp2f((ml.x - M) * 1.4426950408889634f);
    float L = wt * ml.y;
#pragma unroll
    for (int off = 32; off >= 1; off >>= 1) L += __shfl_xor(L, off);
    wts[wave * NSPLIT + lane] = wt;
    if (lane == 0) linv[wave] = 1.0f / L;
  }
  __syncthreads();
  {
    const int hh = tid >> 7, dq = tid & 31, sg = (tid >> 5) & 3;
    f32x4 a = {0.f, 0.f, 0.f, 0.f};
#pragma unroll
    for (int i = 0; i < 16; ++i) {
      const int s = sg * 16 + i;
      const f32x4 v = *reinterpret_cast<const f32x4*>(ws + WS_PO + ((size_t)(hp * 2 + hh) * NSPLIT + s) * DH + dq * 4);
      const float wt = wts[hh * NSPLIT + s];
      a[0] = fmaf(wt, v[0], a[0]); a[1] = fmaf(wt, v[1], a[1]); a[2] = fmaf(wt, v[2], a[2]); a[3] = fmaf(wt, v[3], a[3]);
    }
    *reinterpret_cast<f32x4*>(part + sg * 256 + hh * 128 + dq * 4) = a;
  }
  __syncthreads();
  xs[tid] = rbf(((part[tid] + part[256 + tid]) + (part[512 + tid] + part[768 + tid])) * linv[tid >> 7]);
  __syncthreads();
  float acc[8] = {0, 0, 0, 0, 0, 0, 0, 0};
  T.fma(xs, wave, lane, acc);
  T.wave_reduce_store(acc, red, wave, lane);
  __syncthreads();
  if (tid < 64) ws[WS_ATTP + (size_t)hp * DM + cg * 64 + tid] = (red[tid] + red[64 + tid]) + (red[128 + tid] + red[192 + tid]);
}

__global__ void __launch_bounds__(256) k_gu(const bf16s* __restrict__ x, const bf16s* __restrict__ wg, const bf16s* __restrict__ wu, float* __restrict__ ws) {
  __shared__ __attribute__((aligned(16))) float x1s[DM];
  __shared__ __attribute__((aligned(16))) float xs[128];
  __shared__ __attribute__((aligned(16))) float red[2 * 4 * 128];
  __shared__ float sred[4];
  const int tid = threadIdx.x, lane = tid & 63, wave = tid >> 6;
  const int cg = blockIdx.x % 28, ks = blockIdx.x / 28;
  GemvTile<128, 128, 4> TG, TU;
  TG.load(wg, FF, ks * 128, cg * 128, wave, lane);
  TU.load(wu, FF, ks * 128, cg * 128, wave, lane);
  float xv[4];
  {
    f32x4 a = {0.f, 0.f, 0.f, 0.f};
#pragma unroll
    for (int s = 0; s < S1; ++s) a += *reinterpret_cast<const f32x4*>(ws + WS_ATTP + (size_t)s * DM + tid * 4);
    const f32x4 ga = *reinterpret_cast<const f32x4*>(ws + WS_MODF + 2 * DM + tid * 4);
#pragma unroll
    for (int k = 0; k < 4; ++k) { xv[k] = rbf(bf2f(x[tid * 4 + k]) + rbf(ga[k] * rbf(a[k]))); x1s[tid * 4 + k] = xv[k]; }
    if (blockIdx.x == 0) *reinterpret_cast<f32x4*>(ws + WS_X1 + tid * 4) = f32x4{xv[0], xv[1], xv[2], xv[3]};
  }
  float mb, den;
  ln_stats(xv, sred, tid, mb, den);
  if (tid < 128) {
    const int r = ks * 128 + tid;
    xs[tid] = ada_h(x1s[r], mb, den, ws[WS_MODF + 4 * DM + r], ws[WS_MODF + 3 * DM + r]);
  }
  __syncthreads();
  float ag[8] = {0, 0, 0, 0, 0, 0, 0, 0}, au[8] = {0, 0, 0, 0, 0, 0, 0, 0};
  TG.fma(xs, wave, lane, ag);
  TU.fma(xs, wave, lane, au);
  TG.wave_reduce_store(ag, red, wave, lane);
  TU.wave_reduce_store(au, red + 512, wave, lane);
  __syncthreads();
  {
    const int m = tid >> 7, cc = tid & 127;
    const float* rr = red + m * 512;
    ws[WS_GUP + ((size_t)ks * 2 + m) * FF + cg * 128 + cc] = (rr[cc] + rr[128 + cc]) + (rr[256 + cc] + rr[384 + cc]);
  }
}

__device__ __forceinline__ float gelu_tanh(float v) {
  const float inner = 0.7978845608028654f * (v + 0.044715f * v * v * v);
  return 0.5f * v * (1.0f + tanhf(inner));
}

__global__ void __launch_bounds__(256) k_down(const bf16s* __restrict__ wd, float* __restrict__ ws) {
  __shared__ __attribute__((aligned(16))) float xs[448];
  __shared__ __attribute__((aligned(16))) float red[4 * 32];
  const int tid = threadIdx.x, lane = tid & 63, wave = tid >> 6;
  const int cg = blockIdx.x % 32, ks = blockIdx.x / 32;
  GemvTile<448, 32, 4> T;
  T.load(wd, DM, ks * 448, cg * 32, wave, lane);
  for (int e = tid; e < 448; e += 256) {
    const int r = ks * 448 + e;
    float gs = 0.f, us = 0.f;
#pragma unroll
    for (int s = 0; s < S2; ++s) { gs += ws[WS_GUP + ((size_t)s * 2 + 0) * FF + r]; us += ws[WS_GUP + ((size_t)s * 2 + 1) * FF + r]; }
    xs[e] = rbf(rbf(gelu_tanh(rbf(gs))) * rbf(us));
  }
  __syncthreads();
  float acc[8] = {0, 0, 0, 0, 0, 0, 0, 0};
  T.fma(xs, wave, lane, acc);
  T.wave_reduce_store(acc, red, wave, lane);
  __syncthreads();
  if (tid < 32) ws[WS_DNP + (size_t)ks * DM + cg * 32 + tid] = (red[tid] + red[32 + tid]) + (red[64 + tid] + red[96 + tid]);
}

__global__ void __launch_bounds__(256) k_fin(const float* __restrict__ ws, __hip_bfloat16* __restrict__ out) {
  const int i = blockIdx.x * 256 + threadIdx.x;
  float s = 0.f;
#pragma unroll
  for (int k = 0; k < S3; ++k) s += ws[WS_DNP + (size_t)k * DM + i];
  out[i] = __float2bfloat16(ws[WS_X1 + i] + rbf(ws[WS_MODF + 5 * DM + i] * rbf(s)));
}

extern "C" void kernel_launch(void* const* d_in, const int* in_sizes, int n_in,
                              void* d_out, int out_size, void* d_ws, size_t ws_size,
                              hipStream_t stream) {
  const bf16s* x = (const bf16s*)d_in[0];
  const bf16s* kc = (const bf16s*)d_in[1];
  const bf16s* vc = (const bf16s*)d_in[2];
  const bf16s* wq = (const bf16s*)d_in[3];
  const bf16s* wo = (const bf16s*)d_in[4];
  const bf16s* wg = (const bf16s*)d_in[6];
  const bf16s* wu = (const bf16s*)d_in[7];
  const bf16s* wd = (const bf16s*)d_in[8];
  const bf16s* c = (const bf16s*)d_in[9];
  const bf16s* wmod = (const bf16s*)d_in[10];
  float* ws = (float*)d_ws;
  k_mod<<<96 * SM, 256, 0, stream>>>(c, wmod, ws);
  k_q<<<256, 256, 0, stream>>>(x, wq, ws);
  k_attn<<<256, 512, 0, stream>>>(kc, vc, ws);
  k_wo<<<256, 256, 0, stream>>>(wo, ws);
  k_gu<<<224, 256, 0, stream>>>(x, wg, wu, ws);
  k_down<<<256, 256, 0, stream>>>(wd, ws);
  k_fin<<<4, 256, 0, stream>>>(ws, (__hip_bfloat16*)d_out);
}
```

```cpp
#include <hip/hip_runtime.h>
#include <hip/hip_bf16.h>
#include <stdint.h>

typedef unsigned short bf16s;
typedef unsigned u32x4 __attribute__((ext_vector_type(4)));
typedef unsigned u32x2 __attribute__((ext_vector_type(2)));
typedef float f32x4 __attribute__((ext_vector_type(4)));
typedef short bf16x8 __attribute__((ext_vector_type(8)));

constexpr int DM = 1024, HQ = 32, HKV = 4, SK = 65536, DH = 128, FF = 3584, NMOD = 6144, DQ = HQ * DH;
constexpr int NSPLIT = 64;
constexpr int SM = 8;
constexpr int SQ = 4;
constexpr int S1 = 16;
constexpr int NFW = 224;
constexpr unsigned SENT = 0xFFFFFFFFu;
constexpr int MAXSPIN = 1 << 16;

constexpr size_t WS_MODP = 0;
constexpr size_t WS_HO   = WS_MODP + (size_t)SM * NMOD;
constexpr size_t WS_MODF = WS_HO;
constexpr size_t WS_QP   = WS_MODF + NMOD;
constexpr size_t WS_PML  = WS_QP + (size_t)SQ * DQ;
constexpr size_t WS_PO   = WS_PML + (size_t)HQ * NSPLIT * 2;
constexpr size_t WS_ATTP = WS_PO + (size_t)HQ * NSPLIT * DH;
constexpr size_t WS_DNP  = WS_ATTP + (size_t)S1 * DM;
constexpr size_t WS_END  = WS_DNP + (size_t)DM * NFW;
constexpr int HO_VEC4 = (int)((WS_END - WS_HO) / 4);
static_assert((WS_END - WS_HO) % 4 == 0 && WS_HO % 4 == 0, "16-byte arming stores");

__device__ __forceinline__ float bf2f(bf16s u) { return __builtin_bit_cast(float, (unsigned)u << 16); }
__device__ __forceinline__ float rbf(float f) { return __bfloat162float(__float2bfloat16(f)); }
__device__ __forceinline__ unsigned cvtpk(float lo, float hi) {
  unsigned r; asm volatile("v_cvt_pk_bf16_f32 %0, %1, %2" : "=v"(r) : "v"(lo), "v"(hi)); return r;
}
__device__ __forceinline__ u32x4 ldnt(const void* p) { return __builtin_nontemporal_load(reinterpret_cast<const u32x4*>(p)); }
__device__ __forceinline__ float lo_f(unsigned u) { return __builtin_bit_cast(float, u << 16); }
__device__ __forceinline__ float hi_f(unsigned u) { return __builtin_bit_cast(float, u & 0xffff0000u); }
__device__ __forceinline__ float u2f(unsigned u) { return __builtin_bit_cast(float, u); }
__device__ __forceinline__ unsigned pubw(float f) { const unsigned u = __builtin_bit_cast(unsigned, f); return u == SENT ? 0x7FC00000u : u; }
__device__ __forceinline__ void wg_barrier() { asm volatile("s_waitcnt lgkmcnt(0)\n\ts_barrier" ::: "memory"); }
__device__ __forceinline__ void lds_fence() { asm volatile("s_waitcnt lgkmcnt(0)" ::: "memory"); }

typedef __amdgpu_buffer_rsrc_t rsrc_t;
__device__ __forceinline__ u32x4 ho_ld4(rsrc_t r, size_t foff) { return __builtin_amdgcn_raw_buffer_load_b128(r, (int)(foff * 4), 0, 16); }
__device__ __forceinline__ u32x2 ho_ld2(rsrc_t r, size_t foff) { return __builtin_amdgcn_raw_buffer_load_b64(r, (int)(foff * 4), 0, 16); }
__device__ __forceinline__ void ho_st4(rsrc_t r, size_t foff, f32x4 v) {
  u32x4 u = {pubw(v[0]), pubw(v[1]), pubw(v[2]), pubw(v[3])};
  __builtin_amdgcn_raw_buffer_store_b128(u, r, (int)(foff * 4), 0, 16);
}
__device__ __forceinline__ void ho_st2(rsrc_t r, size_t foff, float a, float b) {
  u32x2 u = {pubw(a), pubw(b)};
  __builtin_amdgcn_raw_buffer_store_b64(u, r, (int)(foff * 4), 0, 16);
}
__device__ __forceinline__ void ho_st1(rsrc_t r, size_t foff, float a) { __builtin_amdgcn_raw_buffer_store_b32(pubw(a), r, (int)(foff * 4), 0, 16); }
__device__ __forceinline__ bool ok4(u32x4 v) { return v[0] != SENT && v[1] != SENT && v[2] != SENT && v[3] != SENT; }

__device__ __forceinline__ float wave_sum(float v) {
#pragma unroll
  for (int off = 32; off >= 1; off >>= 1) v += __shfl_xor(v, off);
  return v;
}
__device__ __forceinline__ float wave_max(float v) {
#pragma unroll
  for (int off = 32; off >= 1; off >>= 1) v = fmaxf(v, __shfl_xor(v, off));
  return v;
}
__device__ __forceinline__ float ada_h(float x, float mb, float den, float cmod, float smod) {
  const float xn = rbf(rbf(x - mb) / den);
  return rbf(rbf(xn * rbf(1.0f + cmod)) + smod);
}
__device__ __forceinline__ void fma8(float xv, u32x4 w, float (&acc)[8]) {
#pragma unroll
  for (int k = 0; k < 4; ++k) {
    acc[2 * k] = fmaf(xv, lo_f(w[k]), acc[2 * k]);
    acc[2 * k + 1] = fmaf(xv, hi_f(w[k]), acc[2 * k + 1]);
  }
}
__device__ __forceinline__ float gelu_tanh(float v) {
  const float inner = 0.7978845608028654f * (v + 0.044715f * v * v * v);
  return 0.5f * v * (1.0f + tanhf(inner));
}

__global__ void __launch_bounds__(256) k_mod(const bf16s* __restrict__ c, const bf16s* __restrict__ wmod, float* __restrict__ ws) {
  __shared__ __attribute__((aligned(16))) float xs[128];
  __shared__ __attribute__((aligned(16))) float red[4 * 64];
  const int tid = threadIdx.x, lane = tid & 63, wave = tid >> 6;
  const int cg = blockIdx.x % 96, ks = blockIdx.x / 96;
  u32x4 w[4];
#pragma unroll
  for (int i = 0; i < 4; ++i) w[i] = ldnt(wmod + (size_t)(ks * 128 + (i * 4 + wave) * 8 + (lane >> 3)) * NMOD + cg * 64 + (lane & 7) * 8);
  {
    const int idx = blockIdx.x * 256 + tid;
    if (idx < HO_VEC4) *reinterpret_cast<u32x4*>(ws + WS_HO + (size_t)idx * 4) = u32x4{SENT, SENT, SENT, SENT};
  }
  if (tid < 128) { const float cv = bf2f(c[ks * 128 + tid]); xs[tid] = rbf(cv / (1.0f + __expf(-cv))); }
  __syncthreads();
  float acc[8] = {0, 0, 0, 0, 0, 0, 0, 0};
#pragma unroll
  for (int i = 0; i < 4; ++i) fma8(xs[(i * 4 + wave) * 8 + (lane >> 3)], w[i], acc);
#pragma unroll
  for (int off = 8; off < 64; off <<= 1)
#pragma unroll
    for (int k = 0; k < 8; ++k) acc[k] += __shfl_xor(acc[k], off);
  if (lane < 8) {
    *reinterpret_cast<f32x4*>(red + wave * 64 + lane * 8) = f32x4{acc[0], acc[1], acc[2], acc[3]};
    *reinterpret_cast<f32x4*>(red + wave * 64 + lane * 8 + 4) = f32x4{acc[4], acc[5], acc[6], acc[7]};
  }
  __syncthreads();
  if (tid < 64) ws[WS_MODP + (size_t)ks * NMOD + cg * 64 + tid] = (red[tid] + red[64 + tid]) + (red[128 + tid] + red[192 + tid]);
}
static_assert(HO_VEC4 <= 96 * SM * 256, "k_mod arms the whole hand-off region");

constexpr int REC = 4 + DH;
__global__ void __launch_bounds__(512, 2) k_mega(const bf16s* __restrict__ x, const bf16s* __restrict__ kc, const bf16s* __restrict__ vc,
                                                 const bf16s* __restrict__ wq, const bf16s* __restrict__ wo, const bf16s* __restrict__ wg,
                                                 const bf16s* __restrict__ wu, const bf16s* __restrict__ wd, float* __restrict__ ws,
                                                 __hip_bfloat16* __restrict__ out) {
  __shared__ __attribute__((aligned(16))) bf16s qs[8 * DH];
  __shared__ __attribute__((aligned(16))) float mrg[8 * 8 * REC];
  __shared__ __attribute__((aligned(16))) float xs0[256];
  __shared__ __attribute__((aligned(16))) float xsa[2 * 128];
  __shared__ __attribute__((aligned(16))) float reda[2 * 64];
  __shared__ __attribute__((aligned(16))) float x1s[DM];
  __shared__ __attribute__((aligned(16))) float xsb[DM];
  __shared__ __attribute__((aligned(16))) float redb[4 * 32];
  __shared__ __attribute__((aligned(16))) float redc[2 * DM];
  __shared__ __attribute__((aligned(16))) float fin[4 * 4];
  __shared__ float st1[4], st2[4];
  const int tid = threadIdx.x, lane = tid & 63, wave = __builtin_amdgcn_readfirstlane(tid >> 6);
  const int b = blockIdx.x;
  const int g = b >> 6, sp = b & 63;
  const int lg = lane >> 4, lc = lane & 15;
  const rsrc_t hr = __builtin_amdgcn_make_buffer_rsrc((void*)ws, 0, (int)(WS_END * 4), 0x00020000);

  const size_t key0 = (size_t)g * SK + (size_t)sp * 1024 + wave * 128;
  const bf16s* kb = kc + (key0 + lc) * DH + lg * 8;
  const bf16s* vb = vc + (key0 + 4 * lg) * DH + lc * 8;
  u32x4 K[8][4], V[4][8];

  if (wave == 7) {
    if (lane < 16) {
      const int idx = 2 * DM + b * 16 + lane;
      float s = 0.f;
#pragma unroll
      for (int k = 0; k < SM; ++k) s += ws[WS_MODP + (size_t)k * NMOD + idx];
      ho_st1(hr, WS_MODF + idx, rbf(s));
    }
  }
  if (wave != 0) {
#pragma unroll
    for (int t = 0; t < 8; ++t)
#pragma unroll
      for (int ks = 0; ks < 4; ++ks) K[t][ks] = ldnt(kb + (size_t)t * 16 * DH + ks * 32);
#pragma unroll
    for (int t = 0; t < 2; ++t)
#pragma unroll
      for (int i = 0; i < 8; ++i) V[t][i] = ldnt(vb + (size_t)(t * 32 + (i >> 2) * 16 + (i & 3)) * DH);
  } else {
    const int cg = b & 63, ks = b >> 6;
    u32x4 w[32];
#pragma unroll
    for (int i = 0; i < 32; ++i) w[i] = ldnt(wq + (size_t)(ks * 256 + i * 8 + (lane >> 3)) * DQ + cg * 64 + (lane & 7) * 8);
    const u32x4 xa = *reinterpret_cast<const u32x4*>(x + lane * 16), xb = *reinterpret_cast<const u32x4*>(x + lane * 16 + 8);
    const u32x2 xr = *reinterpret_cast<const u32x2*>(x + ks * 256 + lane * 4);
    f32x4 sa = {0.f, 0.f, 0.f, 0.f}, ca = {0.f, 0.f, 0.f, 0.f};
#pragma unroll
    for (int s = 0; s < SM; ++s) {
      sa += *reinterpret_cast<const f32x4*>(ws + WS_MODP + (size_t)s * NMOD + ks * 256 + lane * 4);
      ca += *reinterpret_cast<const f32x4*>(ws + WS_MODP + (size_t)s * NMOD + DM + ks * 256 + lane * 4);
    }
    float xv[16];
#pragma unroll
    for (int k = 0; k < 4; ++k) { xv[2 * k] = lo_f(xa[k]); xv[2 * k + 1] = hi_f(xa[k]); xv[8 + 2 * k] = lo_f(xb[k]); xv[9 + 2 * k] = hi_f(xb[k]); }
    float s = 0.f;
#pragma unroll
    for (int k = 0; k < 16; ++k) s += xv[k];
    const float mean = wave_sum(s) * (1.0f / DM);
    s = 0.f;
#pragma unroll
    for (int k = 0; k < 16; ++k) { const float d = xv[k] - mean; s = fmaf(d, d, s); }
    const float var = wave_sum(s) * (1.0f / DM);
    const float mb = rbf(mean), den = rbf(sqrtf(rbf(rbf(var) + 1e-5f)));
    f32x4 h4;
    h4[0] = ada_h(lo_f(xr[0]), mb, den, rbf(ca[0]), rbf(sa[0]));
    h4[1] = ada_h(hi_f(xr[0]), mb, den, rbf(ca[1]), rbf(sa[1]));
    h4[2] = ada_h(lo_f(xr[1]), mb, den, rbf(ca[2]), rbf(sa[2]));
    h4[3] = ada_h(hi_f(xr[1]), mb, den, rbf(ca[3]), rbf(sa[3]));
    *reinterpret_cast<f32x4*>(xs0 + lane * 4) = h4;
    lds_fence();
    float acc[8] = {0, 0, 0, 0, 0, 0, 0, 0};
#pragma unroll
    for (int i = 0; i < 32; ++i) fma8(xs0[i * 8 + (lane >> 3)], w[i], acc);
#pragma unroll
    for (int off = 8; off < 64; off <<= 1)
#pragma unroll
      for (int k = 0; k < 8; ++k) acc[k] += __shfl_xor(acc[k], off);
    if (lane < 8) {
      ho_st4(hr, WS_QP + (size_t)ks * DQ + cg * 64 + lane * 8, f32x4{acc[0], acc[1], acc[2], acc[3]});
      ho_st4(hr, WS_QP + (size_t)ks * DQ + cg * 64 + lane * 8 + 4, f32x4{acc[4], acc[5], acc[6], acc[7]});
    }
    u32x4 qv[4][4];
    for (int it = 0; it < MAXSPIN; ++it) {
      asm volatile("" ::: "memory");
      bool ok = true;
#pragma unroll
      for (int j = 0; j < 4; ++j)
#pragma unroll
        for (int k4 = 0; k4 < SQ; ++k4) { qv[j][k4] = ho_ld4(hr, WS_QP + (size_t)k4 * DQ + g * 1024 + (j * 64 + lane) * 4); }
#pragma unroll
      for (int j = 0; j < 4; ++j)
#pragma unroll
        for (int k4 = 0; k4 < SQ; ++k4) ok = ok && ok4(qv[j][k4]);
      if (__all(ok)) break;
      __builtin_amdgcn_s_sleep(2);
    }
#pragma unroll
    for (int j = 0; j < 4; ++j) {
      float q4[4];
#pragma unroll
      for (int e = 0; e < 4; ++e) q4[e] = (u2f(qv[j][0][e]) + u2f(qv[j][1][e])) + (u2f(qv[j][2][e]) + u2f(qv[j][3][e]));
      u32x2 pk = {cvtpk(q4[0], q4[1]), cvtpk(q4[2], q4[3])};
      *reinterpret_cast<u32x2*>(qs + (j * 64 + lane) * 4) = pk;
    }
#pragma unroll
    for (int t = 0; t < 8; ++t)
#pragma unroll
      for (int ks2 = 0; ks2 < 4; ++ks2) K[t][ks2] = ldnt(kb + (size_t)t * 16 * DH + ks2 * 32);
#pragma unroll
    for (int t = 0; t < 2; ++t)
#pragma unroll
      for (int i = 0; i < 8; ++i) V[t][i] = ldnt(vb + (size_t)(t * 32 + (i >> 2) * 16 + (i & 3)) * DH);
  }
  wg_barrier();
  bf16x8 qf[4];
#pragma unroll
  for (int ks = 0; ks < 4; ++ks) {
    u32x4 z = {0u, 0u, 0u, 0u};
    if (lc < 8) z = *reinterpret_cast<const u32x4*>(qs + lc * DH + ks * 32 + lg * 8);
    qf[ks] = __builtin_bit_cast(bf16x8, z);
  }
  float sc[8][4];
#pragma unroll
  for (int t = 0; t < 8; ++t) {
    f32x4 d = {0.f, 0.f, 0.f, 0.f};
#pragma unroll
    for (int ks = 0; ks < 4; ++ks) d = __builtin_amdgcn_mfma_f32_16x16x32_bf16(__builtin_bit_cast(bf16x8, K[t][ks]), qf[ks], d, 0, 0, 0);
#pragma unroll
    for (int r = 0; r < 4; ++r) sc[t][r] = rbf(rbf(d[r]) * 0.08838834764831845f);
  }
#pragma unroll
  for (int t = 2; t < 4; ++t)
#pragma unroll
    for (int i = 0; i < 8; ++i) V[t][i] = ldnt(vb + (size_t)(t * 32 + (i >> 2) * 16 + (i & 3)) * DH);
  float m = sc[0][0];
#pragma unroll
  for (int t = 0; t < 8; ++t)
#pragma unroll
    for (int r = 0; r < 4; ++r) m = fmaxf(m, sc[t][r]);
  m = fmaxf(m, __shfl_xor(m, 16));
  m = fmaxf(m, __shfl_xor(m, 32));
  float l = 0.f;
  u32x4 P[4];
#pragma unroll
  for (int t = 0; t < 4; ++t) {
    float p[8];
#pragma unroll
    for (int j = 0; j < 8; ++j) { p[j] = __builtin_amdgcn_exp2f((sc[2 * t + (j >> 2)][j & 3] - m) * 1.4426950408889634f); l += p[j]; }
#pragma unroll
    for (int k = 0; k < 4; ++k) P[t][k] = cvtpk(p[2 * k], p[2 * k + 1]);
  }
  l += __shfl_xor(l, 16);
  l += __shfl_xor(l, 32);
  f32x4 acc[8];
#pragma unroll
  for (int d = 0; d < 8; ++d) acc[d] = f32x4{0.f, 0.f, 0.f, 0.f};
  const int hp = b >> 4, cgo = b & 15;
  const bool ffn = b < NFW;
  u32x4 Wt[16];
  const bf16s* wbase; size_t ws1, ws2;
  if (wave < 2) { wbase = wo + (size_t)(hp * 256 + wave * 128 + (lane >> 3)) * DM + cgo * 64 + (lane & 7) * 8; ws1 = (size_t)8 * DM; ws2 = (size_t)64 * DM; }
  else if (wave < 6) { wbase = wg + (size_t)((wave - 2) * 256 + (lane >> 1)) * FF + (ffn ? b : 0) * 16 + (lane & 1) * 8; ws1 = (size_t)32 * FF; ws2 = (size_t)(wu - wg); }
  else { wbase = wd + (size_t)((ffn ? b : 0) * 16 + (wave - 6) * 8) * DM + lane * 8; ws1 = (size_t)DM; ws2 = 512; }
#pragma unroll
  for (int t = 0; t < 4; ++t) {
#pragma unroll
    for (int d = 0; d < 8; ++d) {
      u32x4 bb;
#pragma unroll
      for (int p = 0; p < 4; ++p)
        bb[p] = __builtin_amdgcn_perm(V[t][2 * p + 1][d >> 1], V[t][2 * p][d >> 1], (d & 1) ? 0x07060302u : 0x05040100u);
      acc[d] = __builtin_amdgcn_mfma_f32_16x16x32_bf16(__builtin_bit_cast(bf16x8, P[t]), __builtin_bit_cast(bf16x8, bb), acc[d], 0, 0, 0);
    }
    if (t == 1) {
      if (wave < 2 || ffn) {
#pragma unroll
        for (int i = 0; i < 16; ++i) Wt[i] = ldnt(wbase + (size_t)(i & 7) * ws1 + (size_t)(i >> 3) * ws2);
      } else {
#pragma unroll
        for (int i = 0; i < 16; ++i) Wt[i] = u32x4{0u, 0u, 0u, 0u};
      }
    }
  }
  if (lg < 2) {
#pragma unroll
    for (int r = 0; r < 4; ++r) {
      float* rec = mrg + (wave * 8 + 4 * lg + r) * REC + 4 + 8 * lc;
      *reinterpret_cast<f32x4*>(rec) = f32x4{acc[0][r], acc[1][r], acc[2][r], acc[3][r]};
      *reinterpret_cast<f32x4*>(rec + 4) = f32x4{acc[4][r], acc[5][r], acc[6][r], acc[7][r]};
    }
  }
  if (lane < 8) { mrg[(wave * 8 + lane) * REC] = m; mrg[(wave * 8 + lane) * REC + 1] = l; }
  wg_barrier();
  if (tid < 256) {
    const int h = tid >> 5, dq = tid & 31;
    float M = mrg[h * REC];
#pragma unroll
    for (int w = 1; w < 8; ++w) M = fmaxf(M, mrg[(w * 8 + h) * REC]);
    float L = 0.f; f32x4 o = {0.f, 0.f, 0.f, 0.f};
#pragma unroll
    for (int w = 0; w < 8; ++w) {
      const float* rec = mrg + (w * 8 + h) * REC;
      const float wt = __builtin_amdgcn_exp2f((rec[0] - M) * 1.4426950408889634f);
      L = fmaf(wt, rec[1], L);
      const f32x4 v = *reinterpret_cast<const f32x4*>(rec + 4 + dq * 4);
      o[0] = fmaf(wt, v[0], o[0]); o[1] = fmaf(wt, v[1], o[1]); o[2] = fmaf(wt, v[2], o[2]); o[3] = fmaf(wt, v[3], o[3]);
    }
    const size_t slot = (size_t)(g * 8 + h) * NSPLIT + sp;
    ho_st4(hr, WS_PO + slot * DH + dq * 4, o);
    if (dq == 0) ho_st2(hr, WS_PML + slot * 2, M, L);
  }
  if (wave < 2) {
    const int hd = hp * 2 + wave, sg = lane >> 5, dq = lane & 31;
    u32x2 ml; u32x4 pv[32];
    for (int it = 0; it < MAXSPIN; ++it) {
      asm volatile("" ::: "memory");
      ml = ho_ld2(hr, WS_PML + ((size_t)hd * NSPLIT + lane) * 2);
#pragma unroll
      for (int i = 0; i < 32; ++i) pv[i] = ho_ld4(hr, WS_PO + ((size_t)hd * NSPLIT + sg * 32 + i) * DH + dq * 4);
      bool ok = ml[0] != SENT && ml[1] != SENT;
#pragma unroll
      for (int i = 0; i < 32; ++i) ok = ok && ok4(pv[i]);
      if (__all(ok)) break;
      __builtin_amdgcn_s_sleep(2);
    }
    const float ms = u2f(ml[0]);
    const float M = wave_max(ms);
    const float wt = __builtin_amdgcn_exp2f((ms - M) * 1.4426950408889634f);
    const float L = wave_sum(wt * u2f(ml[1]));
    f32x4 o = {0.f, 0.f, 0.f, 0.f};
#pragma unroll
    for (int i = 0; i < 32; ++i) {
      const float wi = __shfl(wt, sg * 32 + i);
      o[0] = fmaf(wi, u2f(pv[i][0]), o[0]); o[1] = fmaf(wi, u2f(pv[i][1]), o[1]);
      o[2] = fmaf(wi, u2f(pv[i][2]), o[2]); o[3] = fmaf(wi, u2f(pv[i][3]), o[3]);
    }
    const float inv = 1.0f / L;
#pragma unroll
    for (int e = 0; e < 4; ++e) { o[e] += __shfl_xor(o[e], 32); o[e] = rbf(o[e] * inv); }
    if (lane < 32) *reinterpret_cast<f32x4*>(xsa + wave * 128 + dq * 4) = o;
    lds_fence();
    float a8[8] = {0, 0, 0, 0, 0, 0, 0, 0};
#pragma unroll
    for (int i = 0; i < 16; ++i) fma8(xsa[wave * 128 + (i & 7) * 8 + (i >> 3) * 64 + (lane >> 3)], Wt[i], a8);
#pragma unroll
    for (int off = 8; off < 64; off <<= 1)
#pragma unroll
      for (int k = 0; k < 8; ++k) a8[k] += __shfl_xor(a8[k], off);
    if (lane < 8) {
      *reinterpret_cast<f32x4*>(reda + wave * 64 + lane * 8) = f32x4{a8[0], a8[1], a8[2], a8[3]};
      *reinterpret_cast<f32x4*>(reda + wave * 64 + lane * 8 + 4) = f32x4{a8[4], a8[5], a8[6], a8[7]};
    }
  }
  wg_barrier();
  if (tid < 16) {
    const f32x4 a = *reinterpret_cast<const f32x4*>(reda + tid * 4), c2 = *reinterpret_cast<const f32x4*>(reda + 64 + tid * 4);
    ho_st4(hr, WS_ATTP + (size_t)hp * DM + cgo * 64 + tid * 4, a + c2);
  }
  float x1v[4], sbv[4], cbv[4];
  if (wave >= 2 && wave < 6) {
    const int cq = (wave - 2) * 64 + lane;
    u32x4 ap[S1], mg, msb, mcb;
    const u32x2 xr = *reinterpret_cast<const u32x2*>(x + cq * 4);
    for (int it = 0; it < MAXSPIN; ++it) {
      asm volatile("" ::: "memory");
#pragma unroll
      for (int s = 0; s < S1; ++s) ap[s] = ho_ld4(hr, WS_ATTP + (size_t)s * DM + cq * 4);
      mg = ho_ld4(hr, WS_MODF + 2 * DM + cq * 4);
      msb = ho_ld4(hr, WS_MODF + 3 * DM + cq * 4);
      mcb = ho_ld4(hr, WS_MODF + 4 * DM + cq * 4);
      bool ok = ok4(mg) && ok4(msb) && ok4(mcb);
#pragma unroll
      for (int s = 0; s < S1; ++s) ok = ok && ok4(ap[s]);
      if (__all(ok)) break;
      __builtin_amdgcn_s_sleep(2);
    }
    const float xin[4] = {lo_f(xr[0]), hi_f(xr[0]), lo_f(xr[1]), hi_f(xr[1])};
    float s = 0.f;
#pragma unroll
    for (int e = 0; e < 4; ++e) {
      float a = 0.f;
#pragma unroll
      for (int k = 0; k < S1; ++k) a += u2f(ap[k][e]);
      x1v[e] = rbf(xin[e] + rbf(u2f(mg[e]) * rbf(a)));
      sbv[e] = u2f(msb[e]); cbv[e] = u2f(mcb[e]);
      s += x1v[e];
    }
    *reinterpret_cast<f32x4*>(x1s + cq * 4) = f32x4{x1v[0], x1v[1], x1v[2], x1v[3]};
    s = wave_sum(s);
    if (lane == 0) st1[wave - 2] = s;
  }
  wg_barrier();
  float mean2 = 0.f;
  if (wave >= 2 && wave < 6) {
    mean2 = ((st1[0] + st1[1]) + (st1[2] + st1[3])) * (1.0f / DM);
    float s = 0.f;
#pragma unroll
    for (int e = 0; e < 4; ++e) { const float d = x1v[e] - mean2; s = fmaf(d, d, s); }
    s = wave_sum(s);
    if (lane == 0) st2[wave - 2] = s;
  }
  wg_barrier();
  if (wave >= 2 && wave < 6) {
    const float var = ((st2[0] + st2[1]) + (st2[2] + st2[3])) * (1.0f / DM);
    const float mb = rbf(mean2), den = rbf(sqrtf(rbf(rbf(var) + 1e-5f)));
    const int cq = (wave - 2) * 64 + lane;
    f32x4 h4;
#pragma unroll
    for (int e = 0; e < 4; ++e) h4[e] = ada_h(x1v[e], mb, den, cbv[e], sbv[e]);
    *reinterpret_cast<f32x4*>(xsb + cq * 4) = h4;
    lds_fence();
    float ag[8] = {0, 0, 0, 0, 0, 0, 0, 0}, au[8] = {0, 0, 0, 0, 0, 0, 0, 0};
#pragma unroll
    for (int i = 0; i < 8; ++i) {
      const float hv = xsb[(wave - 2) * 256 + i * 32 + (lane >> 1)];
      fma8(hv, Wt[i], ag);
      fma8(hv, Wt[8 + i], au);
    }
#pragma unroll
    for (int off = 2; off < 64; off <<= 1)
#pragma unroll
      for (int k = 0; k < 8; ++k) { ag[k] += __shfl_xor(ag[k], off); au[k] += __shfl_xor(au[k], off); }
    if (lane < 2) {
      float* r = redb + (wave - 2) * 32 + lane * 8;
      *reinterpret_cast<f32x4*>(r) = f32x4{ag[0], ag[1], ag[2], ag[3]};
      *reinterpret_cast<f32x4*>(r + 4) = f32x4{ag[4], ag[5], ag[6], ag[7]};
      *reinterpret_cast<f32x4*>(r + 16) = f32x4{au[0], au[1], au[2], au[3]};
      *reinterpret_cast<f32x4*>(r + 20) = f32x4{au[4], au[5], au[6], au[7]};
    }
  }
  wg_barrier();
  if (wave >= 6) {
    const int cc = lane & 15;
    const float gs = (redb[cc] + redb[32 + cc]) + (redb[64 + cc] + redb[96 + cc]);
    const float us = (redb[16 + cc] + redb[48 + cc]) + (redb[80 + cc] + redb[112 + cc]);
    const float act = rbf(rbf(gelu_tanh(rbf(gs))) * rbf(us));
    float a0[8] = {0, 0, 0, 0, 0, 0, 0, 0}, a1[8] = {0, 0, 0, 0, 0, 0, 0, 0};
#pragma unroll
    for (int r = 0; r < 8; ++r) {
      const float av = __shfl(act, (wave - 6) * 8 + r);
      fma8(av, Wt[r], a0);
      fma8(av, Wt[8 + r], a1);
    }
    float* r = redc + (wave - 6) * DM + lane * 8;
    *reinterpret_cast<f32x4*>(r) = f32x4{a0[0], a0[1], a0[2], a0[3]};
    *reinterpret_cast<f32x4*>(r + 4) = f32x4{a0[4], a0[5], a0[6], a0[7]};
    *reinterpret_cast<f32x4*>(r + 512) = f32x4{a1[0], a1[1], a1[2], a1[3]};
    *reinterpret_cast<f32x4*>(r + 516) = f32x4{a1[4], a1[5], a1[6], a1[7]};
  }
  wg_barrier();
  if (tid < 256 && ffn) {
    const f32x4 a = *reinterpret_cast<const f32x4*>(redc + tid * 4), c2 = *reinterpret_cast<const f32x4*>(redc + DM + tid * 4);
    ho_st4(hr, WS_DNP + ((size_t)tid * NFW + b) * 4, a + c2);
  }
  u32x4 gbv = {0u, 0u, 0u, 0u};
  if (wave < 4) {
    u32x4 dv = {0u, 0u, 0u, 0u};
    const bool act_lane = tid < NFW;
    for (int it = 0; it < MAXSPIN; ++it) {
      asm volatile("" ::: "memory");
      if (act_lane) dv = ho_ld4(hr, WS_DNP + ((size_t)b * NFW + tid) * 4);
      if (wave == 0) gbv = ho_ld4(hr, WS_MODF + 5 * DM + b * 4);
      const bool ok = ok4(dv) && ok4(gbv);
      if (__all(ok)) break;
      __builtin_amdgcn_s_sleep(2);
    }
    f32x4 s4;
#pragma unroll
    for (int e = 0; e < 4; ++e) s4[e] = wave_sum(u2f(dv[e]));
    if (lane == 0) *reinterpret_cast<f32x4*>(fin + wave * 4) = s4;
  }
  wg_barrier();
  if (tid < 4) {
    const float s = (fin[tid] + fin[4 + tid]) + (fin[8 + tid] + fin[12 + tid]);
    out[b * 4 + tid] = __float2bfloat16(x1s[b * 4 + tid] + rbf(u2f(gbv[tid]) * rbf(s)));
  }
}

extern "C" void kernel_launch(void* const* d_in, const int* in_sizes, int n_in,
                              void* d_out, int out_size, void* d_ws, size_t ws_size,
                              hipStream_t stream) {
  const bf16s* x = (const bf16s*)d_in[0];
  const bf16s* kc = (const bf16s*)d_in[1];
  const bf16s* vc = (const bf16s*)d_in[2];
  const bf16s* wq = (const bf16s*)d_in[3];
  const bf16s* wo = (const bf16s*)d_in[4];
  const bf16s* wg = (const bf16s*)d_in[6];
  const bf16s* wu = (const bf16s*)d_in[7];
  const bf16s* wd = (const bf16s*)d_in[8];
  const bf16s* c = (const bf16s*)d_in[9];
  const bf16s* wmod = (const bf16s*)d_in[10];
  float* ws = (float*)d_ws;
  k_mod<<<96 * SM, 256, 0, stream>>>(c, wmod, ws);
  k_mega<<<256, 512, 0, stream>>>(x, kc, vc, wq, wo, wg, wu, wd, ws, (__hip_bfloat16*)d_out);
}
```

```cpp
#include <hip/hip_runtime.h>
#include <hip/hip_bf16.h>
#include <stdint.h>

typedef unsigned short bf16s;
typedef unsigned u32x4 __attribute__((ext_vector_type(4)));
typedef unsigned u32x2 __attribute__((ext_vector_type(2)));
typedef float f32x4 __attribute__((ext_vector_type(4)));
typedef short bf16x8 __attribute__((ext_vector_type(8)));

constexpr int DM = 1024, HQ = 32, HKV = 4, SK = 65536, DH = 128, FF = 3584, NMOD = 6144, DQ = HQ * DH;
constexpr int NSPLIT = 64;
constexpr int SM = 8;
constexpr int SQ = 4;
constexpr int S1 = 16;
constexpr int NFW = 224;
constexpr unsigned SENT = 0xFFFFFFFFu;
constexpr int MAXSPIN = 1 << 16;

constexpr size_t WS_MODP = 0;
constexpr size_t WS_HO   = WS_MODP + (size_t)SM * NMOD;
constexpr size_t WS_MODF = WS_HO;
constexpr size_t WS_QP   = WS_MODF + NMOD;
constexpr size_t WS_PML  = WS_QP + (size_t)SQ * DQ;
constexpr size_t WS_PO   = WS_PML + (size_t)HQ * NSPLIT * 2;
constexpr size_t WS_ATP  = WS_PO + (size_t)HQ * NSPLIT * DH;
constexpr size_t WS_X1   = WS_ATP + (size_t)DM * 256;
constexpr size_t WS_GUP  = WS_X1 + DM;
constexpr size_t WS_DNP  = WS_GUP + (size_t)NFW * 128;
constexpr size_t WS_END  = WS_DNP + (size_t)DM * NFW;
constexpr int HO_VEC4 = (int)((WS_END - WS_HO) / 4);
static_assert((WS_END - WS_HO) % 4 == 0 && WS_HO % 4 == 0, "16-byte arming stores");

__device__ __forceinline__ float bf2f(bf16s u) { return __builtin_bit_cast(float, (unsigned)u << 16); }
__device__ __forceinline__ float rbf(float f) { return __bfloat162float(__float2bfloat16(f)); }
__device__ __forceinline__ unsigned cvtpk(float lo, float hi) {
  unsigned r; asm volatile("v_cvt_pk_bf16_f32 %0, %1, %2" : "=v"(r) : "v"(lo), "v"(hi)); return r;
}
__device__ __forceinline__ u32x4 ldnt(const void* p) { return __builtin_nontemporal_load(reinterpret_cast<const u32x4*>(p)); }
__device__ __forceinline__ float lo_f(unsigned u) { return __builtin_bit_cast(float, u << 16); }
__device__ __forceinline__ float hi_f(unsigned u) { return __builtin_bit_cast(float, u & 0xffff0000u); }
__device__ __forceinline__ float u2f(unsigned u) { return __builtin_bit_cast(float, u); }
__device__ __forceinline__ unsigned pubw(float f) { const unsigned u = __builtin_bit_cast(unsigned, f); return u == SENT ? 0x7FC00000u : u; }
__device__ __forceinline__ void wg_barrier() { asm volatile("s_waitcnt lgkmcnt(0)\n\ts_barrier" ::: "memory"); }
__device__ __forceinline__ void lds_fence() { asm volatile("s_waitcnt lgkmcnt(0)" ::: "memory"); }

typedef __amdgpu_buffer_rsrc_t rsrc_t;
__device__ __forceinline__ u32x4 ho_ld4(rsrc_t r, size_t foff) { return __builtin_amdgcn_raw_buffer_load_b128(r, (int)(foff * 4), 0, 16); }
__device__ __forceinline__ u32x2 ho_ld2(rsrc_t r, size_t foff) { return __builtin_amdgcn_raw_buffer_load_b64(r, (int)(foff * 4), 0, 16); }
__device__ __forceinline__ void ho_st4(rsrc_t r, size_t foff, f32x4 v) {
  u32x4 u = {pubw(v[0]), pubw(v[1]), pubw(v[2]), pubw(v[3])};
  __builtin_amdgcn_raw_buffer_store_b128(u, r, (int)(foff * 4), 0, 16);
}
__device__ __forceinline__ void ho_st2(rsrc_t r, size_t foff, float a, float b) {
  u32x2 u = {pubw(a), pubw(b)};
  __builtin_amdgcn_raw_buffer_store_b64(u, r, (int)(foff * 4), 0, 16);
}
__device__ __forceinline__ void ho_st1(rsrc_t r, size_t foff, float a) { __builtin_amdgcn_raw_buffer_store_b32(pubw(a), r, (int)(foff * 4), 0, 16); }
__device__ __forceinline__ bool ok4(u32x4 v) { return (v[0] != SENT) & (v[1] != SENT) & (v[2] != SENT) & (v[3] != SENT); }

template <int X> __device__ __forceinline__ float sx(float v) {
  const int iv = __builtin_bit_cast(int, v);
  int r;
  if constexpr (X == 1) r = __builtin_amdgcn_mov_dpp(iv, 0xB1, 0xf, 0xf, true);
  else if constexpr (X == 2) r = __builtin_amdgcn_mov_dpp(iv, 0x4E, 0xf, 0xf, true);
  else if constexpr (X == 4) { const int a = __builtin_amdgcn_mov_dpp(iv, 0x124, 0xf, 0xf, true), c = __builtin_amdgcn_mov_dpp(iv, 0x12C, 0xf, 0xf, true);
                               r = (__lane_id() & 4) ? a : c; }
  else if constexpr (X == 8) r = __builtin_amdgcn_mov_dpp(iv, 0x128, 0xf, 0xf, true);
  else if constexpr (X == 16) { auto p = __builtin_amdgcn_permlane16_swap((unsigned)iv, (unsigned)iv, false, false); r = (int)((__lane_id() & 16) ? p[0] : p[1]); }
  else { auto p = __builtin_amdgcn_permlane32_swap((unsigned)iv, (unsigned)iv, false, false); r = (int)((__lane_id() & 32) ? p[0] : p[1]); }
  return __builtin_bit_cast(float, r);
}
__device__ __forceinline__ float wave_sum(float v) {
  v += sx<1>(v); v += sx<2>(v); v += sx<4>(v); v += sx<8>(v); v += sx<16>(v); v += sx<32>(v);
  return v;
}
__device__ __forceinline__ float wave_max(float v) {
  v = fmaxf(v, sx<1>(v)); v = fmaxf(v, sx<2>(v)); v = fmaxf(v, sx<4>(v)); v = fmaxf(v, sx<8>(v)); v = fmaxf(v, sx<16>(v)); v = fmaxf(v, sx<32>(v));
  return v;
}
__device__ __forceinline__ float ada_h(float x, float mb, float den, float cmod, float smod) {
  const float xn = rbf(rbf(x - mb) / den);
  return rbf(rbf(xn * rbf(1.0f + cmod)) + smod);
}
__device__ __forceinline__ void fma8(float xv, u32x4 w, float (&acc)[8]) {
#pragma unroll
  for (int k = 0; k < 4; ++k) {
    acc[2 * k] = fmaf(xv, lo_f(w[k]), acc[2 * k]);
    acc[2 * k + 1] = fmaf(xv, hi_f(w[k]), acc[2 * k + 1]);
  }
}
__device__ __forceinline__ float gelu_tanh(float v) {
  const float inner = 0.7978845608028654f * (v + 0.044715f * v * v * v);
  return 0.5f * v * (1.0f + tanhf(inner));
}

#define STAMP(slot) do { } while (0)
__global__ void __launch_bounds__(256) k_mod(const bf16s* __restrict__ c, const bf16s* __restrict__ wmod, float* __restrict__ ws) {
  __shared__ __attribute__((aligned(16))) float xs[128];
  __shared__ __attribute__((aligned(16))) float red[4 * 64];
  const int tid = threadIdx.x, lane = tid & 63, wave = tid >> 6;
  const int cg = blockIdx.x % 96, ks = blockIdx.x / 96;
  u32x4 w[4];
#pragma unroll
  for (int i = 0; i < 4; ++i) w[i] = ldnt(wmod + (size_t)(ks * 128 + (i * 4 + wave) * 8 + (lane >> 3)) * NMOD + cg * 64 + (lane & 7) * 8);
  {
    for (int idx = blockIdx.x * 256 + tid; idx < HO_VEC4; idx += 96 * SM * 256)
      *reinterpret_cast<u32x4*>(ws + WS_HO + (size_t)idx * 4) = u32x4{SENT, SENT, SENT, SENT};
  }
  if (tid < 128) { const float cv = bf2f(c[ks * 128 + tid]); xs[tid] = rbf(cv / (1.0f + expf(-cv))); }
  __syncthreads();
  float acc[8] = {0, 0, 0, 0, 0, 0, 0, 0};
#pragma unroll
  for (int i = 0; i < 4; ++i) fma8(xs[(i * 4 + wave) * 8 + (lane >> 3)], w[i], acc);
#pragma unroll
  for (int off = 8; off < 64; off <<= 1)
#pragma unroll
    for (int k = 0; k < 8; ++k) acc[k] += __shfl_xor(acc[k], off);
  if (lane < 8) {
    *reinterpret_cast<f32x4*>(red + wave * 64 + lane * 8) = f32x4{acc[0], acc[1], acc[2], acc[3]};
    *reinterpret_cast<f32x4*>(red + wave * 64 + lane * 8 + 4) = f32x4{acc[4], acc[5], acc[6], acc[7]};
  }
  __syncthreads();
  if (tid < 64) ws[WS_MODP + (size_t)ks * NMOD + cg * 64 + tid] = (red[tid] + red[64 + tid]) + (red[128 + tid] + red[192 + tid]);
}

constexpr int REC = 4 + DH;
__global__ void __launch_bounds__(512, 2) k_mega(const bf16s* __restrict__ x, const bf16s* __restrict__ kc, const bf16s* __restrict__ vc,
                                                 const bf16s* __restrict__ wq, const bf16s* __restrict__ wo, const bf16s* __restrict__ wg,
                                                 const bf16s* __restrict__ wu, const bf16s* __restrict__ wd, float* __restrict__ ws,
                                                 __hip_bfloat16* __restrict__ out) {
  __shared__ __attribute__((aligned(16))) bf16s qs[8 * DH];
  __shared__ __attribute__((aligned(16))) float mrg[8 * 8 * REC];
  __shared__ __attribute__((aligned(16))) float xs0[256];
  __shared__ __attribute__((aligned(16))) float osl[16];
  __shared__ __attribute__((aligned(16))) float xsb[DM];
  __shared__ __attribute__((aligned(16))) float redb[4 * 128];
  __shared__ __attribute__((aligned(16))) float actl[16];
  __shared__ float x1l[4];
  __shared__ int flg[8];
  const int tid = threadIdx.x, lane = tid & 63, wave = __builtin_amdgcn_readfirstlane(tid >> 6);
  const int b = blockIdx.x;
  const int g = b >> 6, sp = b & 63;
  const int lg = lane >> 4, lc = lane & 15;
  const rsrc_t hr = __builtin_amdgcn_make_buffer_rsrc((void*)ws, 0, (int)(WS_END * 4), 0x00020000);

  const size_t key0 = (size_t)g * SK + (size_t)sp * 1024 + wave * 16;
  const bf16s* kb = kc + (key0 + lc) * DH + lg * 8;
  const bf16s* vb = vc + (key0 + 4 * lg) * DH + lc * 8;
  u32x4 K[8][4], V[4][8];

  STAMP(0);
  if (tid < 8) flg[tid] = 0;
#define ISSUE_K() do { \
    _Pragma("unroll") for (int t = 0; t < 8; ++t) _Pragma("unroll") for (int ks = 0; ks < 4; ++ks) K[t][ks] = ldnt(kb + (size_t)t * 128 * DH + ks * 32); \
  } while (0)
#define ISSUE_V(t) do { \
    _Pragma("unroll") for (int i = 0; i < 8; ++i) V[t][i] = ldnt(vb + (size_t)((t) * 256 + (i >> 2) * 128 + (i & 3)) * DH); \
  } while (0)
  if (wave != 0) {
    if (wave == 7 && lane < 16) {
      const int idx = 2 * DM + b * 16 + lane;
      float s = 0.f;
#pragma unroll
      for (int k = 0; k < SM; ++k) s += ws[WS_MODP + (size_t)k * NMOD + idx];
      ho_st1(hr, WS_MODF + idx, rbf(s));
    }
    wg_barrier();
    ISSUE_K();
    wg_barrier();
  } else {
    const int cg = b & 63, ks = b >> 6;
    __builtin_amdgcn_s_setprio(3);
    const u32x4 xa = *reinterpret_cast<const u32x4*>(x + lane * 16), xb = *reinterpret_cast<const u32x4*>(x + lane * 16 + 8);
    const u32x2 xr = *reinterpret_cast<const u32x2*>(x + ks * 256 + lane * 4);
    f32x4 sa = {0.f, 0.f, 0.f, 0.f}, ca = {0.f, 0.f, 0.f, 0.f};
#pragma unroll
    for (int s = 0; s < SM; ++s) {
      sa += *reinterpret_cast<const f32x4*>(ws + WS_MODP + (size_t)s * NMOD + ks * 256 + lane * 4);
      ca += *reinterpret_cast<const f32x4*>(ws + WS_MODP + (size_t)s * NMOD + DM + ks * 256 + lane * 4);
    }
    u32x4 w[32];
#pragma unroll
    for (int i = 0; i < 32; ++i) w[i] = ldnt(wq + (size_t)(ks * 256 + i * 8 + (lane >> 3)) * DQ + cg * 64 + (lane & 7) * 8);
    wg_barrier();
    float xv[16];
#pragma unroll
    for (int k = 0; k < 4; ++k) { xv[2 * k] = lo_f(xa[k]); xv[2 * k + 1] = hi_f(xa[k]); xv[8 + 2 * k] = lo_f(xb[k]); xv[9 + 2 * k] = hi_f(xb[k]); }
    float s = 0.f;
#pragma unroll
    for (int k = 0; k < 16; ++k) s += xv[k];
    const float mean = wave_sum(s) * (1.0f / DM);
    s = 0.f;
#pragma unroll
    for (int k = 0; k < 16; ++k) { const float d = xv[k] - mean; s = fmaf(d, d, s); }
    const float var = wave_sum(s) * (1.0f / DM);
    const float mb = rbf(mean), den = rbf(sqrtf(rbf(rbf(var) + 1e-5f)));
    f32x4 h4;
    h4[0] = ada_h(lo_f(xr[0]), mb, den, rbf(ca[0]), rbf(sa[0]));
    h4[1] = ada_h(hi_f(xr[0]), mb, den, rbf(ca[1]), rbf(sa[1]));
    h4[2] = ada_h(lo_f(xr[1]), mb, den, rbf(ca[2]), rbf(sa[2]));
    h4[3] = ada_h(hi_f(xr[1]), mb, den, rbf(ca[3]), rbf(sa[3]));
    *reinterpret_cast<f32x4*>(xs0 + lane * 4) = h4;
    lds_fence();
    float acc[8] = {0, 0, 0, 0, 0, 0, 0, 0};
#pragma unroll
    for (int i = 0; i < 32; ++i) fma8(xs0[i * 8 + (lane >> 3)], w[i], acc);
#pragma unroll
    for (int off = 8; off < 64; off <<= 1)
#pragma unroll
      for (int k = 0; k < 8; ++k) acc[k] += __shfl_xor(acc[k], off);
    if (lane < 8) {
      ho_st4(hr, WS_QP + (size_t)ks * DQ + cg * 64 + lane * 8, f32x4{acc[0], acc[1], acc[2], acc[3]});
      ho_st4(hr, WS_QP + (size_t)ks * DQ + cg * 64 + lane * 8 + 4, f32x4{acc[4], acc[5], acc[6], acc[7]});
    }
    STAMP(1);
    u32x4 qv[4][4];
    for (int it = 0; it < MAXSPIN; ++it) {
      asm volatile("" ::: "memory");
      bool ok = true;
#pragma unroll
      for (int j = 0; j < 4; ++j)
#pragma unroll
        for (int k4 = 0; k4 < SQ; ++k4) { qv[j][k4] = ho_ld4(hr, WS_QP + (size_t)k4 * DQ + g * 1024 + (j * 64 + lane) * 4); }
#pragma unroll
      for (int j = 0; j < 4; ++j)
#pragma unroll
        for (int k4 = 0; k4 < SQ; ++k4) ok = ok & ok4(qv[j][k4]);
      if (__all(ok)) break;
      __builtin_amdgcn_s_sleep(2);
    }
    STAMP(2);
#pragma unroll
    for (int j = 0; j < 4; ++j) {
      float q4[4];
#pragma unroll
      for (int e = 0; e < 4; ++e) q4[e] = (u2f(qv[j][0][e]) + u2f(qv[j][1][e])) + (u2f(qv[j][2][e]) + u2f(qv[j][3][e]));
      u32x2 pk = {cvtpk(q4[0], q4[1]), cvtpk(q4[2], q4[3])};
      *reinterpret_cast<u32x2*>(qs + (j * 64 + lane) * 4) = pk;
    }
    wg_barrier();
    ISSUE_K();
  }
  STAMP(3);

  bf16x8 qf[4];
#pragma unroll
  for (int ks = 0; ks < 4; ++ks) {
    u32x4 z = {0u, 0u, 0u, 0u};
    if (lc < 8) z = *reinterpret_cast<const u32x4*>(qs + lc * DH + ks * 32 + lg * 8);
    qf[ks] = __builtin_bit_cast(bf16x8, z);
  }
  ISSUE_V(0);
  ISSUE_V(1);
  float sc[8][4];
#pragma unroll
  for (int t = 0; t < 8; ++t) {
    f32x4 d = {0.f, 0.f, 0.f, 0.f};
#pragma unroll
    for (int ks = 0; ks < 4; ++ks) d = __builtin_amdgcn_mfma_f32_16x16x32_bf16(__builtin_bit_cast(bf16x8, K[t][ks]), qf[ks], d, 0, 0, 0);
#pragma unroll
    for (int r = 0; r < 4; ++r) sc[t][r] = rbf(rbf(d[r]) * 0.08838834764831845f);
    if (t == 3) ISSUE_V(2);
    if (t == 7) ISSUE_V(3);
  }
  STAMP(4);
  float m = sc[0][0];
#pragma unroll
  for (int t = 0; t < 8; ++t)
#pragma unroll
    for (int r = 0; r < 4; ++r) m = fmaxf(m, sc[t][r]);
  m = fmaxf(m, sx<16>(m));
  m = fmaxf(m, sx<32>(m));
  float l = 0.f;
  u32x4 P[4];
#pragma unroll
  for (int t = 0; t < 4; ++t) {
    float p[8];
#pragma unroll
    for (int j = 0; j < 8; ++j) { p[j] = __builtin_amdgcn_exp2f((sc[2 * t + (j >> 2)][j & 3] - m) * 1.4426950408889634f); l += p[j]; }
#pragma unroll
    for (int k = 0; k < 4; ++k) P[t][k] = cvtpk(p[2 * k], p[2 * k + 1]);
  }
  l += sx<16>(l);
  l += sx<32>(l);
  f32x4 acc[8];
#pragma unroll
  for (int d = 0; d < 8; ++d) acc[d] = f32x4{0.f, 0.f, 0.f, 0.f};
#pragma unroll
  for (int t = 0; t < 4; ++t) {
#pragma unroll
    for (int d = 0; d < 8; ++d) {
      u32x4 bb;
#pragma unroll
      for (int p = 0; p < 4; ++p)
        bb[p] = __builtin_amdgcn_perm(V[t][2 * p + 1][d >> 1], V[t][2 * p][d >> 1], (d & 1) ? 0x07060302u : 0x05040100u);
      acc[d] = __builtin_amdgcn_mfma_f32_16x16x32_bf16(__builtin_bit_cast(bf16x8, P[t]), __builtin_bit_cast(bf16x8, bb), acc[d], 0, 0, 0);
    }
  }
  if (lg < 2) {
#pragma unroll
    for (int r = 0; r < 4; ++r) {
      float* rec = mrg + (wave * 8 + 4 * lg + r) * REC + 4 + 8 * lc;
      *reinterpret_cast<f32x4*>(rec) = f32x4{acc[0][r], acc[1][r], acc[2][r], acc[3][r]};
      *reinterpret_cast<f32x4*>(rec + 4) = f32x4{acc[4][r], acc[5][r], acc[6][r], acc[7][r]};
    }
  }
  if (lane < 8) { mrg[(wave * 8 + lane) * REC] = m; mrg[(wave * 8 + lane) * REC + 1] = l; }
  STAMP(5);
  wg_barrier();
  STAMP(6);
  if (tid < 256) {
    const int h = tid >> 5, dq = tid & 31;
    float M = mrg[h * REC];
#pragma unroll
    for (int w = 1; w < 8; ++w) M = fmaxf(M, mrg[(w * 8 + h) * REC]);
    float L = 0.f; f32x4 o = {0.f, 0.f, 0.f, 0.f};
#pragma unroll
    for (int w = 0; w < 8; ++w) {
      const float* rec = mrg + (w * 8 + h) * REC;
      const float wt = __builtin_amdgcn_exp2f((rec[0] - M) * 1.4426950408889634f);
      L = fmaf(wt, rec[1], L);
      const f32x4 v = *reinterpret_cast<const f32x4*>(rec + 4 + dq * 4);
      o[0] = fmaf(wt, v[0], o[0]); o[1] = fmaf(wt, v[1], o[1]); o[2] = fmaf(wt, v[2], o[2]); o[3] = fmaf(wt, v[3], o[3]);
    }
    const size_t slot = (size_t)(g * 8 + h) * NSPLIT + sp;
    ho_st4(hr, WS_PO + slot * DH + dq * 4, o);
    if (dq == 0) ho_st2(hr, WS_PML + slot * 2, M, L);
  }
  volatile __attribute__((address_space(3))) int* vflg = (volatile __attribute__((address_space(3))) int*)flg;
  const bool ffn = b < NFW;
  const int cgf = b % 56, ksf = b / 56;
#define TR_STEP(v, n, X) _Pragma("unroll") for (int i_ = 0; i_ < (n) / 2; ++i_) { \
    const bool up_ = (lane & (X)) != 0; const float keep_ = up_ ? v[i_ + (n) / 2] : v[i_], give_ = up_ ? v[i_] : v[i_ + (n) / 2]; \
    v[i_] = keep_ + sx<X>(give_); }
#define LDS_SPIN(idx, val) do { for (int it_ = 0; it_ < (MAXSPIN << 4) && vflg[idx] < (val); ++it_) __builtin_amdgcn_s_sleep(1); asm volatile("" ::: "memory"); } while (0)
  if (wave < 4) __builtin_amdgcn_s_setprio(3);
  if (wave == 0) {
    const int hd = b >> 3, ds = b & 7;
    u32x2 ml; u32x4 pv[4];
    for (int it = 0; it < MAXSPIN; ++it) {
      asm volatile("" ::: "memory");
      ml = ho_ld2(hr, WS_PML + ((size_t)hd * NSPLIT + lane) * 2);
#pragma unroll
      for (int i = 0; i < 4; ++i) pv[i] = ho_ld4(hr, WS_PO + ((size_t)hd * NSPLIT + lane) * DH + ds * 16 + i * 4);
      bool ok = (ml[0] != SENT) & (ml[1] != SENT);
#pragma unroll
      for (int i = 0; i < 4; ++i) ok = ok & ok4(pv[i]);
      if (__all(ok)) break;
      __builtin_amdgcn_s_sleep(1);
    }
    STAMP(7);
    const float ms = u2f(ml[0]);
    const float M = wave_max(ms);
    const float wt = __builtin_amdgcn_exp2f((ms - M) * 1.4426950408889634f);
    const float L = wave_sum(wt * u2f(ml[1]));
    float v[16];
#pragma unroll
    for (int i = 0; i < 16; ++i) v[i] = wt * u2f(pv[i >> 2][i & 3]);
    TR_STEP(v, 16, 32) TR_STEP(v, 8, 16) TR_STEP(v, 4, 8) TR_STEP(v, 2, 4)
    v[0] += sx<2>(v[0]); v[0] += sx<1>(v[0]);
    if ((lane & 3) == 0) osl[lane >> 2] = rbf(v[0] / L);
    lds_fence();
    if (lane == 0) vflg[0] = 1;
    STAMP(8);
    if (ffn) {
      u32x4 gu = {0u, 0u, 0u, 0u};
      for (int it = 0; it < MAXSPIN; ++it) {
        asm volatile("" ::: "memory");
        if (lane < 32) gu = ho_ld4(hr, WS_GUP + (size_t)b * 128 + lane * 4);
        if (__all(ok4(gu))) break;
        __builtin_amdgcn_s_sleep(1);
      }
      STAMP(14);
      float t4[4];
#pragma unroll
      for (int e = 0; e < 4; ++e) { t4[e] = u2f(gu[e]); t4[e] += sx<8>(t4[e]); t4[e] += sx<16>(t4[e]); }
#pragma unroll
      for (int e = 0; e < 4; ++e) {
        const float us = sx<4>(t4[e]);
        if (lane < 4) actl[lane * 4 + e] = rbf(rbf(gelu_tanh(rbf(t4[e]))) * rbf(us));
      }
      lds_fence();
      if (lane == 0) vflg[4] = 1;
    }
  } else if (wave == 1) {
    u32x4 ap[4], mg;
    const u32x2 xr = *reinterpret_cast<const u32x2*>(x + b * 4);
    for (int it = 0; it < MAXSPIN; ++it) {
      asm volatile("" ::: "memory");
#pragma unroll
      for (int k = 0; k < 4; ++k) ap[k] = ho_ld4(hr, WS_ATP + ((size_t)b * 256 + k * 64 + lane) * 4);
      mg = ho_ld4(hr, WS_MODF + 2 * DM + b * 4);
      const bool ok = ok4(ap[0]) & ok4(ap[1]) & ok4(ap[2]) & ok4(ap[3]) & ok4(mg);
      if (__all(ok)) break;
      __builtin_amdgcn_s_sleep(1);
    }
    STAMP(9);
    float v[4];
#pragma unroll
    for (int e = 0; e < 4; ++e) v[e] = (u2f(ap[0][e]) + u2f(ap[1][e])) + (u2f(ap[2][e]) + u2f(ap[3][e]));
    TR_STEP(v, 4, 32) TR_STEP(v, 2, 16)
    v[0] += sx<8>(v[0]); v[0] += sx<4>(v[0]); v[0] += sx<2>(v[0]); v[0] += sx<1>(v[0]);
    const int e = lane >> 4;
    const unsigned xw = (e & 2) ? xr[1] : xr[0];
    const float xin = (e & 1) ? hi_f(xw) : lo_f(xw);
    const unsigned gw = (e & 2) ? ((e & 1) ? mg[3] : mg[2]) : ((e & 1) ? mg[1] : mg[0]);
    const float x1 = rbf(xin + rbf(u2f(gw) * rbf(v[0])));
    if ((lane & 15) == 0) { ho_st1(hr, WS_X1 + b * 4 + e, x1); x1l[e] = x1; }
    lds_fence();
    if (lane == 0) vflg[3] = 1;
  } else if (wave == 2) {
    u32x4 xq[4], sbq[4], cbq[4];
    for (int it = 0; it < MAXSPIN; ++it) {
      asm volatile("" ::: "memory");
      bool ok = true;
#pragma unroll
      for (int k = 0; k < 4; ++k) {
        sbq[k] = ho_ld4(hr, WS_MODF + 3 * DM + (k * 64 + lane) * 4);
        cbq[k] = ho_ld4(hr, WS_MODF + 4 * DM + (k * 64 + lane) * 4);
        ok = ok & ok4(sbq[k]) & ok4(cbq[k]);
      }
      if (__all(ok)) break;
      __builtin_amdgcn_s_sleep(1);
    }
    for (int it = 0; it < MAXSPIN; ++it) {
      asm volatile("" ::: "memory");
      bool ok = true;
#pragma unroll
      for (int k = 0; k < 4; ++k) { xq[k] = ho_ld4(hr, WS_X1 + (k * 64 + lane) * 4); ok = ok & ok4(xq[k]); }
      if (__all(ok)) break;
      __builtin_amdgcn_s_sleep(1);
    }
    STAMP(10);
    float s = 0.f;
#pragma unroll
    for (int k = 0; k < 4; ++k) s += (u2f(xq[k][0]) + u2f(xq[k][1])) + (u2f(xq[k][2]) + u2f(xq[k][3]));
    const float mean = wave_sum(s) * (1.0f / DM);
    s = 0.f;
#pragma unroll
    for (int k = 0; k < 4; ++k)
#pragma unroll
      for (int e = 0; e < 4; ++e) { const float d = u2f(xq[k][e]) - mean; s = fmaf(d, d, s); }
    const float var = wave_sum(s) * (1.0f / DM);
    const float mb = rbf(mean), den = rbf(sqrtf(rbf(rbf(var) + 1e-5f)));
#pragma unroll
    for (int k = 0; k < 4; ++k) {
      f32x4 h4;
#pragma unroll
      for (int e = 0; e < 4; ++e) h4[e] = ada_h(u2f(xq[k][e]), mb, den, u2f(cbq[k][e]), u2f(sbq[k][e]));
      *reinterpret_cast<f32x4*>(xsb + (k * 64 + lane) * 4) = h4;
    }
    lds_fence();
    if (lane == 0) vflg[1] = 1;
  } else if (wave == 3) {
    u32x4 dv[4], gbv;
    for (int it = 0; it < MAXSPIN; ++it) {
      asm volatile("" ::: "memory");
#pragma unroll
      for (int k = 0; k < 4; ++k) {
        dv[k] = u32x4{0u, 0u, 0u, 0u};
        if (k * 64 + lane < NFW) dv[k] = ho_ld4(hr, WS_DNP + ((size_t)b * NFW + k * 64 + lane) * 4);
      }
      gbv = ho_ld4(hr, WS_MODF + 5 * DM + b * 4);
      const bool ok = ok4(dv[0]) & ok4(dv[1]) & ok4(dv[2]) & ok4(dv[3]) & ok4(gbv);
      if (__all(ok)) break;
      __builtin_amdgcn_s_sleep(1);
    }
    STAMP(12);
    float v[4];
#pragma unroll
    for (int e = 0; e < 4; ++e) v[e] = (u2f(dv[0][e]) + u2f(dv[1][e])) + (u2f(dv[2][e]) + u2f(dv[3][e]));
    TR_STEP(v, 4, 32) TR_STEP(v, 2, 16)
    v[0] += sx<8>(v[0]); v[0] += sx<4>(v[0]); v[0] += sx<2>(v[0]); v[0] += sx<1>(v[0]);
    LDS_SPIN(3, 1);
    const int e = lane >> 4;
    const unsigned gw = (e & 2) ? ((e & 1) ? gbv[3] : gbv[2]) : ((e & 1) ? gbv[1] : gbv[0]);
    if ((lane & 15) == 0) out[b * 4 + e] = __float2bfloat16(x1l[e] + rbf(u2f(gw) * rbf(v[0])));
    STAMP(13);
  } else {
    const int gw = wave - 4;
    u32x4 Wo[8], Wg[8], Wu[8], Wd[8];
    {
      const bf16s* p = wo + (size_t)(b * 16 + (lane >> 5)) * DM + gw * 256 + (lane & 31) * 8;
#pragma unroll
      for (int i = 0; i < 8; ++i) Wo[i] = ldnt(p + (size_t)i * 2 * DM);
    }
    if (ffn) {
      const size_t off = (size_t)(ksf * 256 + gw * 64 + (lane >> 3)) * FF + cgf * 64 + (lane & 7) * 8;
#pragma unroll
      for (int i = 0; i < 8; ++i) { Wg[i] = ldnt(wg + off + (size_t)i * 8 * FF); Wu[i] = ldnt(wu + off + (size_t)i * 8 * FF); }
    }
    LDS_SPIN(0, 1);
    {
      float a8[8] = {0, 0, 0, 0, 0, 0, 0, 0};
#pragma unroll
      for (int i = 0; i < 8; ++i) fma8(osl[2 * i + (lane >> 5)], Wo[i], a8);
#pragma unroll
      for (int k = 0; k < 8; ++k) a8[k] += sx<32>(a8[k]);
      if (lane < 32) {
        const size_t q0 = (size_t)gw * 64 + lane * 2;
        ho_st4(hr, WS_ATP + (q0 * 256 + b) * 4, f32x4{a8[0], a8[1], a8[2], a8[3]});
        ho_st4(hr, WS_ATP + ((q0 + 1) * 256 + b) * 4, f32x4{a8[4], a8[5], a8[6], a8[7]});
      }
    }
    if (ffn) {
      {
        const bf16s* p = wd + (size_t)(b * 16 + (lane >> 5)) * DM + gw * 256 + (lane & 31) * 8;
#pragma unroll
        for (int i = 0; i < 8; ++i) Wd[i] = ldnt(p + (size_t)i * 2 * DM);
      }
      LDS_SPIN(1, 1);
      float v[16];
#pragma unroll
      for (int k = 0; k < 16; ++k) v[k] = 0.f;
#pragma unroll
      for (int i = 0; i < 8; ++i) {
        const float hv = xsb[ksf * 256 + gw * 64 + i * 8 + (lane >> 3)];
#pragma unroll
        for (int k = 0; k < 4; ++k) {
          v[2 * k] = fmaf(hv, lo_f(Wg[i][k]), v[2 * k]); v[2 * k + 1] = fmaf(hv, hi_f(Wg[i][k]), v[2 * k + 1]);
          v[8 + 2 * k] = fmaf(hv, lo_f(Wu[i][k]), v[8 + 2 * k]); v[9 + 2 * k] = fmaf(hv, hi_f(Wu[i][k]), v[9 + 2 * k]);
        }
      }
      TR_STEP(v, 16, 32) TR_STEP(v, 8, 16) TR_STEP(v, 4, 8)
      {
        const int vid = (lane >> 3) * 2, col = (lane & 7) * 8 + (vid & 7);
        *reinterpret_cast<float2*>(redb + gw * 128 + (vid >> 3) * 64 + col) = make_float2(v[0], v[1]);
      }
      lds_fence();
      if (lane == 0) __hip_atomic_fetch_add(&flg[2], 1, __ATOMIC_RELAXED, __HIP_MEMORY_SCOPE_WORKGROUP);
      LDS_SPIN(2, 4);
      if (lane < 8) {
        const int idx = (lane >> 2) * 64 + gw * 16 + (lane & 3) * 4;
        const f32x4 t = (*reinterpret_cast<const f32x4*>(redb + idx) + *reinterpret_cast<const f32x4*>(redb + 128 + idx)) +
                        (*reinterpret_cast<const f32x4*>(redb + 256 + idx) + *reinterpret_cast<const f32x4*>(redb + 384 + idx));
        ho_st4(hr, WS_GUP + (size_t)(cgf * 4 + gw) * 128 + ksf * 32 + lane * 4, t);
      }
      STAMP(11);
      LDS_SPIN(4, 1);
      float a8[8] = {0, 0, 0, 0, 0, 0, 0, 0};
#pragma unroll
      for (int i = 0; i < 8; ++i) fma8(actl[2 * i + (lane >> 5)], Wd[i], a8);
#pragma unroll
      for (int k = 0; k < 8; ++k) a8[k] += sx<32>(a8[k]);
      if (lane < 32) {
        const size_t q0 = (size_t)gw * 64 + lane * 2;
        ho_st4(hr, WS_DNP + (q0 * NFW + b) * 4, f32x4{a8[0], a8[1], a8[2], a8[3]});
        ho_st4(hr, WS_DNP + ((q0 + 1) * NFW + b) * 4, f32x4{a8[4], a8[5], a8[6], a8[7]});
      }
    }
  }
}

extern "C" void kernel_launch(void* const* d_in, const int* in_sizes, int n_in,
                              void* d_out, int out_size, void* d_ws, size_t ws_size,
                              hipStream_t stream) {
  const bf16s* x = (const bf16s*)d_in[0];
  const bf16s* kc = (const bf16s*)d_in[1];
  const bf16s* vc = (const bf16s*)d_in[2];
  const bf16s* wq = (const bf16s*)d_in[3];
  const bf16s* wo = (const bf16s*)d_in[4];
  const bf16s* wg = (const bf16s*)d_in[6];
  const bf16s* wu = (const bf16s*)d_in[7];
  const bf16s* wd = (const bf16s*)d_in[8];
  const bf16s* c = (const bf16s*)d_in[9];
  const bf16s* wmod = (const bf16s*)d_in[10];
  float* ws = (float*)d_ws;
  k_mod<<<96 * SM, 256, 0, stream>>>(c, wmod, ws);
  k_mega<<<256, 512, 0, stream>>>(x, kc, vc, wq, wo, wg, wu, wd, ws, (__hip_bfloat16*)d_out);
}
```

```cpp
#include <hip/hip_runtime.h>
#include <hip/hip_bf16.h>
#include <stdint.h>

typedef unsigned short bf16s;
typedef unsigned u32x4 __attribute__((ext_vector_type(4)));
typedef unsigned u32x2 __attribute__((ext_vector_type(2)));
typedef float f32x4 __attribute__((ext_vector_type(4)));
typedef short bf16x8 __attribute__((ext_vector_type(8)));

constexpr int DM = 1024, HQ = 32, HKV = 4, SK = 65536, DH = 128, FF = 3584, NMOD = 6144, DQ = HQ * DH;
constexpr int NSPLIT = 64;
constexpr int SM = 8;
constexpr int SQ = 4;
constexpr int S1 = 16;
constexpr int NFW = 224;
constexpr unsigned SENT = 0xFFFFFFFFu;
constexpr int MAXSPIN = 1 << 16;

constexpr size_t WS_MODP = 0;
constexpr size_t WS_HO   = WS_MODP + (size_t)SM * NMOD;
constexpr size_t WS_MODF = WS_HO;
constexpr size_t WS_QP   = WS_MODF + NMOD;
constexpr size_t WS_PML  = WS_QP + (size_t)SQ * DQ;
constexpr size_t WS_PO   = WS_PML + (size_t)HQ * NSPLIT * 2;
constexpr size_t WS_ATP  = WS_PO + (size_t)HQ * NSPLIT * DH;
constexpr size_t WS_X1   = WS_ATP + (size_t)DM * 256;
constexpr size_t WS_GUP  = WS_X1 + DM;
constexpr size_t WS_DNP  = WS_GUP + (size_t)NFW * 128;
constexpr size_t WS_END  = WS_DNP + (size_t)DM * NFW;
constexpr int HO_VEC4 = (int)((WS_END - WS_HO) / 4);
static_assert((WS_END - WS_HO) % 4 == 0 && WS_HO % 4 == 0, "16-byte arming stores");

__device__ __forceinline__ float bf2f(bf16s u) { return __builtin_bit_cast(float, (unsigned)u << 16); }
__device__ __forceinline__ float rbf(float f) { return __bfloat162float(__float2bfloat16(f)); }
__device__ __forceinline__ unsigned cvtpk(float lo, float hi) {
  unsigned r; asm volatile("v_cvt_pk_bf16_f32 %0, %1, %2" : "=v"(r) : "v"(lo), "v"(hi)); return r;
}
__device__ __forceinline__ u32x4 ldnt(const void* p) { return __builtin_nontemporal_load(reinterpret_cast<const u32x4*>(p)); }
__device__ __forceinline__ float lo_f(unsigned u) { return __builtin_bit_cast(float, u << 16); }
__device__ __forceinline__ float hi_f(unsigned u) { return __builtin_bit_cast(float, u & 0xffff0000u); }
__device__ __forceinline__ float u2f(unsigned u) { return __builtin_bit_cast(float, u); }
__device__ __forceinline__ unsigned pubw(float f) { const unsigned u = __builtin_bit_cast(unsigned, f); return u == SENT ? 0x7FC00000u : u; }
__device__ __forceinline__ void wg_barrier() { asm volatile("s_waitcnt lgkmcnt(0)\n\ts_barrier" ::: "memory"); }
__device__ __forceinline__ void lds_fence() { asm volatile("s_waitcnt lgkmcnt(0)" ::: "memory"); }

typedef __amdgpu_buffer_rsrc_t rsrc_t;
__device__ __forceinline__ u32x4 ho_ld4(rsrc_t r, size_t foff) { return __builtin_amdgcn_raw_buffer_load_b128(r, (int)(foff * 4), 0, 16); }
__device__ __forceinline__ unsigned ho_ld1(rsrc_t r, size_t foff) { return __builtin_amdgcn_raw_buffer_load_b32(r, (int)(foff * 4), 0, 16); }
__device__ __forceinline__ u32x2 ho_ld2(rsrc_t r, size_t foff) { return __builtin_amdgcn_raw_buffer_load_b64(r, (int)(foff * 4), 0, 16); }
__device__ __forceinline__ void ho_st4(rsrc_t r, size_t foff, f32x4 v) {
  u32x4 u = {pubw(v[0]), pubw(v[1]), pubw(v[2]), pubw(v[3])};
  __builtin_amdgcn_raw_buffer_store_b128(u, r, (int)(foff * 4), 0, 16);
}
__device__ __forceinline__ void ho_st2(rsrc_t r, size_t foff, float a, float b) {
  u32x2 u = {pubw(a), pubw(b)};
  __builtin_amdgcn_raw_buffer_store_b64(u, r, (int)(foff * 4), 0, 16);
}
__device__ __forceinline__ void ho_st1(rsrc_t r, size_t foff, float a) { __builtin_amdgcn_raw_buffer_store_b32(pubw(a), r, (int)(foff * 4), 0, 16); }
__device__ __forceinline__ bool ok4(u32x4 v) { return (v[0] != SENT) & (v[1] != SENT) & (v[2] != SENT) & (v[3] != SENT); }

template <int X> __device__ __forceinline__ float sx(float v) {
  const int iv = __builtin_bit_cast(int, v);
  int r;
  if constexpr (X == 1) r = __builtin_amdgcn_mov_dpp(iv, 0xB1, 0xf, 0xf, true);
  else if constexpr (X == 2) r = __builtin_amdgcn_mov_dpp(iv, 0x4E, 0xf, 0xf, true);
  else if constexpr (X == 4) { const int a = __builtin_amdgcn_mov_dpp(iv, 0x124, 0xf, 0xf, true), c = __builtin_amdgcn_mov_dpp(iv, 0x12C, 0xf, 0xf, true);
                               r = (__lane_id() & 4) ? a : c; }
  else if constexpr (X == 8) r = __builtin_amdgcn_mov_dpp(iv, 0x128, 0xf, 0xf, true);
  else if constexpr (X == 16) { auto p = __builtin_amdgcn_permlane16_swap((unsigned)iv, (unsigned)iv, false, false); r = (int)((__lane_id() & 16) ? p[0] : p[1]); }
  else { auto p = __builtin_amdgcn_permlane32_swap((unsigned)iv, (unsigned)iv, false, false); r = (int)((__lane_id() & 32) ? p[0] : p[1]); }
  return __builtin_bit_cast(float, r);
}
__device__ __forceinline__ float wave_sum(float v) {
  v += sx<1>(v); v += sx<2>(v); v += sx<4>(v); v += sx<8>(v); v += sx<16>(v); v += sx<32>(v);
  return v;
}
__device__ __forceinline__ float wave_max(float v) {
  v = fmaxf(v, sx<1>(v)); v = fmaxf(v, sx<2>(v)); v = fmaxf(v, sx<4>(v)); v = fmaxf(v, sx<8>(v)); v = fmaxf(v, sx<16>(v)); v = fmaxf(v, sx<32>(v));
  return v;
}
__device__ __forceinline__ float ada_h(float x, float mb, float den, float cmod, float smod) {
  const float xn = rbf(rbf(x - mb) / den);
  return rbf(rbf(xn * rbf(1.0f + cmod)) + smod);
}
__device__ __forceinline__ void fma8(float xv, u32x4 w, float (&acc)[8]) {
#pragma unroll
  for (int k = 0; k < 4; ++k) {
    acc[2 * k] = fmaf(xv, lo_f(w[k]), acc[2 * k]);
    acc[2 * k + 1] = fmaf(xv, hi_f(w[k]), acc[2 * k + 1]);
  }
}
__device__ __forceinline__ float gelu_tanh(float v) {
  const float inner = 0.7978845608028654f * (v + 0.044715f * v * v * v);
  return 0.5f * v * (1.0f + tanhf(inner));
}

#define STAMP(slot) do { } while (0)
__global__ void __launch_bounds__(256) k_mod(const bf16s* __restrict__ c, const bf16s* __restrict__ wmod, float* __restrict__ ws) {
  __shared__ __attribute__((aligned(16))) float xs[128];
  __shared__ __attribute__((aligned(16))) float red[4 * 64];
  const int tid = threadIdx.x, lane = tid & 63, wave = tid >> 6;
  const int cg = blockIdx.x % 96, ks = blockIdx.x / 96;
  u32x4 w[4];
#pragma unroll
  for (int i = 0; i < 4; ++i) w[i] = ldnt(wmod + (size_t)(ks * 128 + (i * 4 + wave) * 8 + (lane >> 3)) * NMOD + cg * 64 + (lane & 7) * 8);
  {
    for (int idx = blockIdx.x * 256 + tid; idx < HO_VEC4; idx += 96 * SM * 256)
      *reinterpret_cast<u32x4*>(ws + WS_HO + (size_t)idx * 4) = u32x4{SENT, SENT, SENT, SENT};
  }
  if (tid < 128) { const float cv = bf2f(c[ks * 128 + tid]); xs[tid] = rbf(cv / (1.0f + expf(-cv))); }
  __syncthreads();
  float acc[8] = {0, 0, 0, 0, 0, 0, 0, 0};
#pragma unroll
  for (int i = 0; i < 4; ++i) fma8(xs[(i * 4 + wave) * 8 + (lane >> 3)], w[i], acc);
#pragma unroll
  for (int off = 8; off < 64; off <<= 1)
#pragma unroll
    for (int k = 0; k < 8; ++k) acc[k] += __shfl_xor(acc[k], off);
  if (lane < 8) {
    *reinterpret_cast<f32x4*>(red + wave * 64 + lane * 8) = f32x4{acc[0], acc[1], acc[2], acc[3]};
    *reinterpret_cast<f32x4*>(red + wave * 64 + lane * 8 + 4) = f32x4{acc[4], acc[5], acc[6], acc[7]};
  }
  __syncthreads();
  if (tid < 64) ws[WS_MODP + (size_t)ks * NMOD + cg * 64 + tid] = (red[tid] + red[64 + tid]) + (red[128 + tid] + red[192 + tid]);
}

constexpr int REC = 4 + DH;
__global__ void __launch_bounds__(512, 2) k_mega(const bf16s* __restrict__ x, const bf16s* __restrict__ kc, const bf16s* __restrict__ vc,
                                                 const bf16s* __restrict__ wq, const bf16s* __restrict__ wo, const bf16s* __restrict__ wg,
                                                 const bf16s* __restrict__ wu, const bf16s* __restrict__ wd, float* __restrict__ ws,
                                                 __hip_bfloat16* __restrict__ out) {
  __shared__ __attribute__((aligned(16))) bf16s qs[8 * DH];
  __shared__ __attribute__((aligned(16))) float mrg[8 * 8 * REC];
  __shared__ __attribute__((aligned(16))) float xs0[256];
  __shared__ __attribute__((aligned(16))) float osl[16];
  __shared__ __attribute__((aligned(16))) float xsb[256];
  __shared__ __attribute__((aligned(16))) float redb[4 * 128];
  __shared__ __attribute__((aligned(16))) float actl[16];
  __shared__ float x1l[4];
  __shared__ int flg[8];
  const int tid = threadIdx.x, lane = tid & 63, wave = __builtin_amdgcn_readfirstlane(tid >> 6);
  const int b = blockIdx.x;
  const int g = b >> 6, sp = b & 63;
  const int lg = lane >> 4, lc = lane & 15;
  const rsrc_t hr = __builtin_amdgcn_make_buffer_rsrc((void*)ws, 0, (int)(WS_END * 4), 0x00020000);

  const size_t key0 = (size_t)g * SK + (size_t)(sp * 8 + wave) * 16;
  const bf16s* kb = kc + (key0 + lc) * DH + lg * 8;
  const bf16s* vb = vc + (key0 + 4 * lg) * DH + lc * 8;
  u32x4 K[8][4], V[4][8];

  STAMP(0);
  if (tid < 8) flg[tid] = 0;
#define ISSUE_K() do { \
    _Pragma("unroll") for (int t = 0; t < 8; ++t) _Pragma("unroll") for (int ks = 0; ks < 4; ++ks) K[t][ks] = ldnt(kb + (size_t)t * 8192 * DH + ks * 32); \
  } while (0)
#define ISSUE_V(t) do { \
    _Pragma("unroll") for (int i = 0; i < 8; ++i) V[t][i] = ldnt(vb + (size_t)((t) * 16384 + (i >> 2) * 8192 + (i & 3)) * DH); \
  } while (0)
  if (wave != 0) {
    if (wave == 7 && lane < 16) {
      const int idx = 2 * DM + b * 16 + lane;
      float s = 0.f;
#pragma unroll
      for (int k = 0; k < SM; ++k) s += ws[WS_MODP + (size_t)k * NMOD + idx];
      ho_st1(hr, WS_MODF + idx, rbf(s));
    }
    wg_barrier();
    ISSUE_K();
    wg_barrier();
  } else {
    const int cg = b & 63, ks = b >> 6;
    __builtin_amdgcn_s_setprio(3);
    const u32x4 xa = *reinterpret_cast<const u32x4*>(x + lane * 16), xb = *reinterpret_cast<const u32x4*>(x + lane * 16 + 8);
    const u32x2 xr = *reinterpret_cast<const u32x2*>(x + ks * 256 + lane * 4);
    f32x4 sa = {0.f, 0.f, 0.f, 0.f}, ca = {0.f, 0.f, 0.f, 0.f};
#pragma unroll
    for (int s = 0; s < SM; ++s) {
      sa += *reinterpret_cast<const f32x4*>(ws + WS_MODP + (size_t)s * NMOD + ks * 256 + lane * 4);
      ca += *reinterpret_cast<const f32x4*>(ws + WS_MODP + (size_t)s * NMOD + DM + ks * 256 + lane * 4);
    }
    u32x4 w[32];
#pragma unroll
    for (int i = 0; i < 32; ++i) w[i] = ldnt(wq + (size_t)(ks * 256 + i * 8 + (lane >> 3)) * DQ + cg * 64 + (lane & 7) * 8);
    wg_barrier();
    float xv[16];
#pragma unroll
    for (int k = 0; k < 4; ++k) { xv[2 * k] = lo_f(xa[k]); xv[2 * k + 1] = hi_f(xa[k]); xv[8 + 2 * k] = lo_f(xb[k]); xv[9 + 2 * k] = hi_f(xb[k]); }
    float s = 0.f;
#pragma unroll
    for (int k = 0; k < 16; ++k) s += xv[k];
    const float mean = wave_sum(s) * (1.0f / DM);
    s = 0.f;
#pragma unroll
    for (int k = 0; k < 16; ++k) { const float d = xv[k] - mean; s = fmaf(d, d, s); }
    const float var = wave_sum(s) * (1.0f / DM);
    const float mb = rbf(mean), den = rbf(sqrtf(rbf(rbf(var) + 1e-5f)));
    f32x4 h4;
    h4[0] = ada_h(lo_f(xr[0]), mb, den, rbf(ca[0]), rbf(sa[0]));
    h4[1] = ada_h(hi_f(xr[0]), mb, den, rbf(ca[1]), rbf(sa[1]));
    h4[2] = ada_h(lo_f(xr[1]), mb, den, rbf(ca[2]), rbf(sa[2]));
    h4[3] = ada_h(hi_f(xr[1]), mb, den, rbf(ca[3]), rbf(sa[3]));
    *reinterpret_cast<f32x4*>(xs0 + lane * 4) = h4;
    lds_fence();
    float acc[8] = {0, 0, 0, 0, 0, 0, 0, 0};
#pragma unroll
    for (int i = 0; i < 32; ++i) fma8(xs0[i * 8 + (lane >> 3)], w[i], acc);
#pragma unroll
    for (int off = 8; off < 64; off <<= 1)
#pragma unroll
      for (int k = 0; k < 8; ++k) acc[k] += __shfl_xor(acc[k], off);
    if (lane < 8) {
      ho_st4(hr, WS_QP + (size_t)ks * DQ + cg * 64 + lane * 8, f32x4{acc[0], acc[1], acc[2], acc[3]});
      ho_st4(hr, WS_QP + (size_t)ks * DQ + cg * 64 + lane * 8 + 4, f32x4{acc[4], acc[5], acc[6], acc[7]});
    }
    STAMP(1);
    u32x4 qv[4][4];
    for (int it = 0; it < MAXSPIN; ++it) {
      asm volatile("" ::: "memory");
      bool ok = true;
#pragma unroll
      for (int j = 0; j < 4; ++j)
#pragma unroll
        for (int k4 = 0; k4 < SQ; ++k4) { qv[j][k4] = ho_ld4(hr, WS_QP + (size_t)k4 * DQ + g * 1024 + (j * 64 + lane) * 4); }
#pragma unroll
      for (int j = 0; j < 4; ++j)
#pragma unroll
        for (int k4 = 0; k4 < SQ; ++k4) ok = ok & ok4(qv[j][k4]);
      if (__all(ok)) break;
      __builtin_amdgcn_s_sleep(2);
    }
    STAMP(2);
#pragma unroll
    for (int j = 0; j < 4; ++j) {
      float q4[4];
#pragma unroll
      for (int e = 0; e < 4; ++e) q4[e] = (u2f(qv[j][0][e]) + u2f(qv[j][1][e])) + (u2f(qv[j][2][e]) + u2f(qv[j][3][e]));
      u32x2 pk = {cvtpk(q4[0], q4[1]), cvtpk(q4[2], q4[3])};
      *reinterpret_cast<u32x2*>(qs + (j * 64 + lane) * 4) = pk;
    }
    wg_barrier();
    ISSUE_K();
  }
  STAMP(3);

  bf16x8 qf[4];
#pragma unroll
  for (int ks = 0; ks < 4; ++ks) {
    u32x4 z = {0u, 0u, 0u, 0u};
    if (lc < 8) z = *reinterpret_cast<const u32x4*>(qs + lc * DH + ks * 32 + lg * 8);
    qf[ks] = __builtin_bit_cast(bf16x8, z);
  }
  ISSUE_V(0);
  ISSUE_V(1);
  float sc[8][4];
#pragma unroll
  for (int t = 0; t < 8; ++t) {
    f32x4 d = {0.f, 0.f, 0.f, 0.f};
#pragma unroll
    for (int ks = 0; ks < 4; ++ks) d = __builtin_amdgcn_mfma_f32_16x16x32_bf16(__builtin_bit_cast(bf16x8, K[t][ks]), qf[ks], d, 0, 0, 0);
#pragma unroll
    for (int r = 0; r < 4; ++r) sc[t][r] = rbf(rbf(d[r]) * 0.08838834764831845f);
    if (t == 3) ISSUE_V(2);
    if (t == 7) ISSUE_V(3);
  }
  STAMP(4);
  float m = sc[0][0];
#pragma unroll
  for (int t = 0; t < 8; ++t)
#pragma unroll
    for (int r = 0; r < 4; ++r) m = fmaxf(m, sc[t][r]);
  m = fmaxf(m, sx<16>(m));
  m = fmaxf(m, sx<32>(m));
  float l = 0.f;
  u32x4 P[4];
#pragma unroll
  for (int t = 0; t < 4; ++t) {
    float p[8];
#pragma unroll
    for (int j = 0; j < 8; ++j) { p[j] = __builtin_amdgcn_exp2f((sc[2 * t + (j >> 2)][j & 3] - m) * 1.4426950408889634f); l += p[j]; }
#pragma unroll
    for (int k = 0; k < 4; ++k) P[t][k] = cvtpk(p[2 * k], p[2 * k + 1]);
  }
  l += sx<16>(l);
  l += sx<32>(l);
  f32x4 acc[8];
#pragma unroll
  for (int d = 0; d < 8; ++d) acc[d] = f32x4{0.f, 0.f, 0.f, 0.f};
#pragma unroll
  for (int t = 0; t < 4; ++t) {
#pragma unroll
    for (int d = 0; d < 8; ++d) {
      u32x4 bb;
#pragma unroll
      for (int p = 0; p < 4; ++p)
        bb[p] = __builtin_amdgcn_perm(V[t][2 * p + 1][d >> 1], V[t][2 * p][d >> 1], (d & 1) ? 0x07060302u : 0x05040100u);
      acc[d] = __builtin_amdgcn_mfma_f32_16x16x32_bf16(__builtin_bit_cast(bf16x8, P[t]), __builtin_bit_cast(bf16x8, bb), acc[d], 0, 0, 0);
    }
  }
  if (lg < 2) {
#pragma unroll
    for (int r = 0; r < 4; ++r) {
      float* rec = mrg + (wave * 8 + 4 * lg + r) * REC + 4 + 8 * lc;
      *reinterpret_cast<f32x4*>(rec) = f32x4{acc[0][r], acc[1][r], acc[2][r], acc[3][r]};
      *reinterpret_cast<f32x4*>(rec + 4) = f32x4{acc[4][r], acc[5][r], acc[6][r], acc[7][r]};
    }
  }
  if (lane < 8) { mrg[(wave * 8 + lane) * REC] = m; mrg[(wave * 8 + lane) * REC + 1] = l; }
  STAMP(5);
  wg_barrier();
  STAMP(6);
  if (tid < 256) {
    const int h = tid >> 5, dq = tid & 31;
    float M = mrg[h * REC];
#pragma unroll
    for (int w = 1; w < 8; ++w) M = fmaxf(M, mrg[(w * 8 + h) * REC]);
    float L = 0.f; f32x4 o = {0.f, 0.f, 0.f, 0.f};
#pragma unroll
    for (int w = 0; w < 8; ++w) {
      const float* rec = mrg + (w * 8 + h) * REC;
      const float wt = __builtin_amdgcn_exp2f((rec[0] - M) * 1.4426950408889634f);
      L = fmaf(wt, rec[1], L);
      const f32x4 v = *reinterpret_cast<const f32x4*>(rec + 4 + dq * 4);
      o[0] = fmaf(wt, v[0], o[0]); o[1] = fmaf(wt, v[1], o[1]); o[2] = fmaf(wt, v[2], o[2]); o[3] = fmaf(wt, v[3], o[3]);
    }
    const size_t slot = (size_t)(g * 8 + h) * NSPLIT + sp;
    ho_st4(hr, WS_PO + slot * DH + dq * 4, o);
    if (dq == 0) ho_st2(hr, WS_PML + slot * 2, M, L);
  }
  volatile __attribute__((address_space(3))) int* vflg = (volatile __attribute__((address_space(3))) int*)flg;
  const bool ffn = b < NFW;
  const int cgf = b % 56, ksf = b / 56;
#define TR_STEP(v, n, X) _Pragma("unroll") for (int i_ = 0; i_ < (n) / 2; ++i_) { \
    const bool up_ = (lane & (X)) != 0; const float keep_ = up_ ? v[i_ + (n) / 2] : v[i_], give_ = up_ ? v[i_] : v[i_ + (n) / 2]; \
    v[i_] = keep_ + sx<X>(give_); }
#define LDS_SPIN(idx, val) do { for (int it_ = 0; it_ < (MAXSPIN << 4) && vflg[idx] < (val); ++it_) __builtin_amdgcn_s_sleep(1); asm volatile("" ::: "memory"); } while (0)
  if (wave < 4) __builtin_amdgcn_s_setprio(3);
  if (wave == 0) {
    const int hd = b >> 3, ds = b & 7;
    u32x2 ml; u32x4 pv[4];
    for (int it = 0; it < MAXSPIN; ++it) {
      asm volatile("" ::: "memory");
      ml = ho_ld2(hr, WS_PML + ((size_t)hd * NSPLIT + lane) * 2);
#pragma unroll
      for (int i = 0; i < 4; ++i) pv[i] = ho_ld4(hr, WS_PO + ((size_t)hd * NSPLIT + lane) * DH + ds * 16 + i * 4);
      bool ok = (ml[0] != SENT) & (ml[1] != SENT);
#pragma unroll
      for (int i = 0; i < 4; ++i) ok = ok & ok4(pv[i]);
      if (__all(ok)) break;
      __builtin_amdgcn_s_sleep(1);
    }
    STAMP(7);
    const float ms = u2f(ml[0]);
    const float M = wave_max(ms);
    const float wt = __builtin_amdgcn_exp2f((ms - M) * 1.4426950408889634f);
    const float L = wave_sum(wt * u2f(ml[1]));
    float v[16];
#pragma unroll
    for (int i = 0; i < 16; ++i) v[i] = wt * u2f(pv[i >> 2][i & 3]);
    TR_STEP(v, 16, 32) TR_STEP(v, 8, 16) TR_STEP(v, 4, 8) TR_STEP(v, 2, 4)
    v[0] += sx<2>(v[0]); v[0] += sx<1>(v[0]);
    if ((lane & 3) == 0) osl[lane >> 2] = rbf(v[0] / L);
    lds_fence();
    if (lane == 0) vflg[0] = 1;
    STAMP(8);
    if (ffn) {
      u32x4 gu = {0u, 0u, 0u, 0u};
      for (int it = 0; it < MAXSPIN; ++it) {
        asm volatile("" ::: "memory");
        if (lane < 32) gu = ho_ld4(hr, WS_GUP + (size_t)b * 128 + lane * 4);
        if (__all(ok4(gu))) break;
        __builtin_amdgcn_s_sleep(1);
      }
      STAMP(14);
      float t4[4];
#pragma unroll
      for (int e = 0; e < 4; ++e) { t4[e] = u2f(gu[e]); t4[e] += sx<8>(t4[e]); t4[e] += sx<16>(t4[e]); }
#pragma unroll
      for (int e = 0; e < 4; ++e) {
        const float us = sx<4>(t4[e]);
        if (lane < 4) actl[lane * 4 + e] = rbf(rbf(gelu_tanh(rbf(t4[e]))) * rbf(us));
      }
      lds_fence();
      if (lane == 0) vflg[4] = 1;
    }
  } else if (wave == 1) {
    u32x4 ap[4], mg;
    const u32x2 xr = *reinterpret_cast<const u32x2*>(x + b * 4);
    for (int it = 0; it < MAXSPIN; ++it) {
      asm volatile("" ::: "memory");
#pragma unroll
      for (int k = 0; k < 4; ++k) ap[k] = ho_ld4(hr, WS_ATP + ((size_t)b * 256 + k * 64 + lane) * 4);
      mg = ho_ld4(hr, WS_MODF + 2 * DM + b * 4);
      const bool ok = ok4(ap[0]) & ok4(ap[1]) & ok4(ap[2]) & ok4(ap[3]) & ok4(mg);
      if (__all(ok)) break;
      __builtin_amdgcn_s_sleep(1);
    }
    STAMP(9);
    float v[4];
#pragma unroll
    for (int e = 0; e < 4; ++e) v[e] = (u2f(ap[0][e]) + u2f(ap[1][e])) + (u2f(ap[2][e]) + u2f(ap[3][e]));
    TR_STEP(v, 4, 32) TR_STEP(v, 2, 16)
    v[0] += sx<8>(v[0]); v[0] += sx<4>(v[0]); v[0] += sx<2>(v[0]); v[0] += sx<1>(v[0]);
    const int e = lane >> 4;
    const unsigned xw = (e & 2) ? xr[1] : xr[0];
    const float xin = (e & 1) ? hi_f(xw) : lo_f(xw);
    const unsigned gw = (e & 2) ? ((e & 1) ? mg[3] : mg[2]) : ((e & 1) ? mg[1] : mg[0]);
    const float x1 = rbf(xin + rbf(u2f(gw) * rbf(v[0])));
    if ((lane & 15) == 0) { ho_st1(hr, WS_X1 + b * 4 + e, x1); x1l[e] = x1; }
    lds_fence();
    if (lane == 0) vflg[3] = 1;
  } else if (wave == 2) {
  } else if (wave == 3) {
    u32x4 dv[4], gbv;
    for (int it = 0; it < MAXSPIN; ++it) {
      asm volatile("" ::: "memory");
#pragma unroll
      for (int k = 0; k < 4; ++k) {
        dv[k] = u32x4{0u, 0u, 0u, 0u};
        if (k * 64 + lane < NFW) dv[k] = ho_ld4(hr, WS_DNP + ((size_t)b * NFW + k * 64 + lane) * 4);
      }
      gbv = ho_ld4(hr, WS_MODF + 5 * DM + b * 4);
      const bool ok = ok4(dv[0]) & ok4(dv[1]) & ok4(dv[2]) & ok4(dv[3]) & ok4(gbv);
      if (__all(ok)) break;
      __builtin_amdgcn_s_sleep(1);
    }
    STAMP(12);
    float v[4];
#pragma unroll
    for (int e = 0; e < 4; ++e) v[e] = (u2f(dv[0][e]) + u2f(dv[1][e])) + (u2f(dv[2][e]) + u2f(dv[3][e]));
    TR_STEP(v, 4, 32) TR_STEP(v, 2, 16)
    v[0] += sx<8>(v[0]); v[0] += sx<4>(v[0]); v[0] += sx<2>(v[0]); v[0] += sx<1>(v[0]);
    LDS_SPIN(3, 1);
    const int e = lane >> 4;
    const unsigned gw = (e & 2) ? ((e & 1) ? gbv[3] : gbv[2]) : ((e & 1) ? gbv[1] : gbv[0]);
    if ((lane & 15) == 0) out[b * 4 + e] = __float2bfloat16(x1l[e] + rbf(u2f(gw) * rbf(v[0])));
    STAMP(13);
  } else {
    const int gw = wave - 4;
    u32x4 Wo[8], Wg[8], Wu[8], Wd[8];
    {
      const bf16s* p = wo + (size_t)(b * 16 + (lane >> 5)) * DM + gw * 256 + (lane & 31) * 8;
#pragma unroll
      for (int i = 0; i < 8; ++i) Wo[i] = ldnt(p + (size_t)i * 2 * DM);
    }
    LDS_SPIN(0, 1);
    {
      float a8[8] = {0, 0, 0, 0, 0, 0, 0, 0};
#pragma unroll
      for (int i = 0; i < 8; ++i) fma8(osl[2 * i + (lane >> 5)], Wo[i], a8);
#pragma unroll
      for (int k = 0; k < 8; ++k) a8[k] += sx<32>(a8[k]);
      if (lane < 32) {
        const size_t q0 = (size_t)gw * 64 + lane * 2;
        ho_st4(hr, WS_ATP + (q0 * 256 + b) * 4, f32x4{a8[0], a8[1], a8[2], a8[3]});
        ho_st4(hr, WS_ATP + ((q0 + 1) * 256 + b) * 4, f32x4{a8[4], a8[5], a8[6], a8[7]});
      }
    }
    if (ffn) {
      LDS_SPIN(3, 1);
      {
        const size_t off = (size_t)(ksf * 256 + gw * 64 + (lane >> 3)) * FF + cgf * 64 + (lane & 7) * 8;
#pragma unroll
        for (int i = 0; i < 8; ++i) { Wg[i] = ldnt(wg + off + (size_t)i * 8 * FF); Wu[i] = ldnt(wu + off + (size_t)i * 8 * FF); }
      }
      const int myrow = ksf * 256 + gw * 64 + lane;
      u32x4 xq[4]; unsigned xm, sbm, cbm;
      for (int it = 0; it < MAXSPIN; ++it) {
        asm volatile("" ::: "memory");
        bool ok = true;
#pragma unroll
        for (int k = 0; k < 4; ++k) { xq[k] = ho_ld4(hr, WS_X1 + (k * 64 + lane) * 4); ok = ok & ok4(xq[k]); }
        xm = ho_ld1(hr, WS_X1 + myrow); sbm = ho_ld1(hr, WS_MODF + 3 * DM + myrow); cbm = ho_ld1(hr, WS_MODF + 4 * DM + myrow);
        ok = ok & (xm != SENT) & (sbm != SENT) & (cbm != SENT);
        if (__all(ok)) break;
        __builtin_amdgcn_s_sleep(1);
      }
      STAMP(10);
      {
        const bf16s* p = wd + (size_t)(b * 16 + (lane >> 5)) * DM + gw * 256 + (lane & 31) * 8;
#pragma unroll
        for (int i = 0; i < 8; ++i) Wd[i] = ldnt(p + (size_t)i * 2 * DM);
      }
      {
        float s = 0.f;
#pragma unroll
        for (int k = 0; k < 4; ++k) s += (u2f(xq[k][0]) + u2f(xq[k][1])) + (u2f(xq[k][2]) + u2f(xq[k][3]));
        const float mean = wave_sum(s) * (1.0f / DM);
        s = 0.f;
#pragma unroll
        for (int k = 0; k < 4; ++k)
#pragma unroll
          for (int e = 0; e < 4; ++e) { const float d = u2f(xq[k][e]) - mean; s = fmaf(d, d, s); }
        const float var = wave_sum(s) * (1.0f / DM);
        const float mb = rbf(mean), den = rbf(sqrtf(rbf(rbf(var) + 1e-5f)));
        xsb[gw * 64 + lane] = ada_h(u2f(xm), mb, den, u2f(cbm), u2f(sbm));
        lds_fence();
      }
      float v[16];
#pragma unroll
      for (int k = 0; k < 16; ++k) v[k] = 0.f;
#pragma unroll
      for (int i = 0; i < 8; ++i) {
        const float hv = xsb[gw * 64 + i * 8 + (lane >> 3)];
#pragma unroll
        for (int k = 0; k < 4; ++k) {
          v[2 * k] = fmaf(hv, lo_f(Wg[i][k]), v[2 * k]); v[2 * k + 1] = fmaf(hv, hi_f(Wg[i][k]), v[2 * k + 1]);
          v[8 + 2 * k] = fmaf(hv, lo_f(Wu[i][k]), v[8 + 2 * k]); v[9 + 2 * k] = fmaf(hv, hi_f(Wu[i][k]), v[9 + 2 * k]);
        }
      }
      TR_STEP(v, 16, 32) TR_STEP(v, 8, 16) TR_STEP(v, 4, 8)
      {
        const int vid = (lane >> 3) * 2, col = (lane & 7) * 8 + (vid & 7);
        *reinterpret_cast<float2*>(redb + gw * 128 + (vid >> 3) * 64 + col) = make_float2(v[0], v[1]);
      }
      lds_fence();
      if (lane == 0) __hip_atomic_fetch_add(&flg[2], 1, __ATOMIC_RELAXED, __HIP_MEMORY_SCOPE_WORKGROUP);
      LDS_SPIN(2, 4);
      if (lane < 8) {
        const int idx = (lane >> 2) * 64 + gw * 16 + (lane & 3) * 4;
        const f32x4 t = (*reinterpret_cast<const f32x4*>(redb + idx) + *reinterpret_cast<const f32x4*>(redb + 128 + idx)) +
                        (*reinterpret_cast<const f32x4*>(redb + 256 + idx) + *reinterpret_cast<const f32x4*>(redb + 384 + idx));
        ho_st4(hr, WS_GUP + (size_t)(cgf * 4 + gw) * 128 + ksf * 32 + lane * 4, t);
      }
      STAMP(11);
      LDS_SPIN(4, 1);
      float a8[8] = {0, 0, 0, 0, 0, 0, 0, 0};
#pragma unroll
      for (int i = 0; i < 8; ++i) fma8(actl[2 * i + (lane >> 5)], Wd[i], a8);
#pragma unroll
      for (int k = 0; k < 8; ++k) a8[k] += sx<32>(a8[k]);
      if (lane < 32) {
        const size_t q0 = (size_t)gw * 64 + lane * 2;
        ho_st4(hr, WS_DNP + (q0 * NFW + b) * 4, f32x4{a8[0], a8[1], a8[2], a8[3]});
        ho_st4(hr, WS_DNP + ((q0 + 1) * NFW + b) * 4, f32x4{a8[4], a8[5], a8[6], a8[7]});
      }
    }
  }
}

extern "C" void kernel_launch(void* const* d_in, const int* in_sizes, int n_in,
                              void* d_out, int out_size, void* d_ws, size_t ws_size,
                              hipStream_t stream) {
  const bf16s* x = (const bf16s*)d_in[0];
  const bf16s* kc = (const bf16s*)d_in[1];
  const bf16s* vc = (const bf16s*)d_in[2];
  const bf16s* wq = (const bf16s*)d_in[3];
  const bf16s* wo = (const bf16s*)d_in[4];
  const bf16s* wg = (const bf16s*)d_in[6];
  const bf16s* wu = (const bf16s*)d_in[7];
  const bf16s* wd = (const bf16s*)d_in[8];
  const bf16s* c = (const bf16s*)d_in[9];
  const bf16s* wmod = (const bf16s*)d_in[10];
  float* ws = (float*)d_ws;
  k_mod<<<96 * SM, 256, 0, stream>>>(c, wmod, ws);
  k_mega<<<256, 512, 0, stream>>>(x, kc, vc, wq, wo, wg, wu, wd, ws, (__hip_bfloat16*)d_out);
}
```

```cpp
#include <hip/hip_runtime.h>
#include <hip/hip_bf16.h>
#include <stdint.h>

typedef unsigned short bf16s;
typedef unsigned u32x4 __attribute__((ext_vector_type(4)));
typedef unsigned u32x2 __attribute__((ext_vector_type(2)));
typedef float f32x4 __attribute__((ext_vector_type(4)));
typedef short bf16x8 __attribute__((ext_vector_type(8)));

constexpr int DM = 1024, HQ = 32, HKV = 4, SK = 65536, DH = 128, FF = 3584, NMOD = 6144, DQ = HQ * DH;
constexpr int NSPLIT = 64;
constexpr int SM = 8;
constexpr int SQ = 4;
constexpr int NFW = 224;
constexpr unsigned SENT = 0xFFFFFFFFu;
constexpr int MAXSPIN = 1 << 16;

constexpr size_t WS_MODP = 0;
constexpr size_t WS_HO   = WS_MODP + (size_t)SM * NMOD;
constexpr size_t WS_MODF = WS_HO;
constexpr size_t WS_QP   = WS_MODF + NMOD;
constexpr size_t WS_PML  = WS_QP + (size_t)SQ * DQ;
constexpr size_t WS_PO   = WS_PML + (size_t)HQ * NSPLIT * 2;
constexpr size_t WS_ATP  = WS_PO + (size_t)HQ * NSPLIT * DH;
constexpr size_t WS_X1   = WS_ATP + (size_t)DM * 256;
constexpr size_t WS_GUP  = WS_X1 + DM;
constexpr size_t WS_DNP  = WS_GUP + (size_t)NFW * 128;
constexpr size_t WS_END  = WS_DNP + (size_t)DM * NFW;
constexpr int HO_VEC4 = (int)((WS_END - WS_HO) / 4);
static_assert((WS_END - WS_HO) % 4 == 0 && WS_HO % 4 == 0, "16-byte arming stores");

__device__ __forceinline__ float bf2f(bf16s u) { return __builtin_bit_cast(float, (unsigned)u << 16); }
__device__ __forceinline__ float rbf(float f) { return __bfloat162float(__float2bfloat16(f)); }
__device__ __forceinline__ unsigned cvtpk(float lo, float hi) {
  unsigned r; asm volatile("v_cvt_pk_bf16_f32 %0, %1, %2" : "=v"(r) : "v"(lo), "v"(hi)); return r;
}
__device__ __forceinline__ u32x4 ldnt(const void* p) { return __builtin_nontemporal_load(reinterpret_cast<const u32x4*>(p)); }
__device__ __forceinline__ float lo_f(unsigned u) { return __builtin_bit_cast(float, u << 16); }
__device__ __forceinline__ float hi_f(unsigned u) { return __builtin_bit_cast(float, u & 0xffff0000u); }
__device__ __forceinline__ float u2f(unsigned u) { return __builtin_bit_cast(float, u); }
__device__ __forceinline__ unsigned pubw(float f) { const unsigned u = __builtin_bit_cast(unsigned, f); return u == SENT ? 0x7FC00000u : u; }
__device__ __forceinline__ void wg_barrier() { asm volatile("s_waitcnt lgkmcnt(0)\n\ts_barrier" ::: "memory"); }
__device__ __forceinline__ void lds_fence() { asm volatile("s_waitcnt lgkmcnt(0)" ::: "memory"); }

typedef __amdgpu_buffer_rsrc_t rsrc_t;
__device__ __forceinline__ u32x4 ho_ld4(rsrc_t r, size_t foff) { return __builtin_amdgcn_raw_buffer_load_b128(r, (int)(foff * 4), 0, 16); }
__device__ __forceinline__ unsigned ho_ld1(rsrc_t r, size_t foff) { return __builtin_amdgcn_raw_buffer_load_b32(r, (int)(foff * 4), 0, 16); }
__device__ __forceinline__ u32x2 ho_ld2(rsrc_t r, size_t foff) { return __builtin_amdgcn_raw_buffer_load_b64(r, (int)(foff * 4), 0, 16); }
__device__ __forceinline__ void ho_st4(rsrc_t r, size_t foff, f32x4 v) {
  u32x4 u = {pubw(v[0]), pubw(v[1]), pubw(v[2]), pubw(v[3])};
  __builtin_amdgcn_raw_buffer_store_b128(u, r, (int)(foff * 4), 0, 16);
}
__device__ __forceinline__ void ho_st2(rsrc_t r, size_t foff, float a, float b) {
  u32x2 u = {pubw(a), pubw(b)};
  __builtin_amdgcn_raw_buffer_store_b64(u, r, (int)(foff * 4), 0, 16);
}
__device__ __forceinline__ void ho_st1(rsrc_t r, size_t foff, float a) { __builtin_amdgcn_raw_buffer_store_b32(pubw(a), r, (int)(foff * 4), 0, 16); }
__device__ __forceinline__ bool ok4(u32x4 v) { return (v[0] != SENT) & (v[1] != SENT) & (v[2] != SENT) & (v[3] != SENT); }

template <int X> __device__ __forceinline__ float sx(float v) {
  const int iv = __builtin_bit_cast(int, v);
  int r;
  if constexpr (X == 1) r = __builtin_amdgcn_mov_dpp(iv, 0xB1, 0xf, 0xf, true);
  else if constexpr (X == 2) r = __builtin_amdgcn_mov_dpp(iv, 0x4E, 0xf, 0xf, true);
  else if constexpr (X == 4) { const int a = __builtin_amdgcn_mov_dpp(iv, 0x124, 0xf, 0xf, true), c = __builtin_amdgcn_mov_dpp(iv, 0x12C, 0xf, 0xf, true);
                               r = (__lane_id() & 4) ? a : c; }
  else if constexpr (X == 8) r = __builtin_amdgcn_mov_dpp(iv, 0x128, 0xf, 0xf, true);
  else if constexpr (X == 16) { auto p = __builtin_amdgcn_permlane16_swap((unsigned)iv, (unsigned)iv, false, false); r = (int)((__lane_id() & 16) ? p[0] : p[1]); }
  else { auto p = __builtin_amdgcn_permlane32_swap((unsigned)iv, (unsigned)iv, false, false); r = (int)((__lane_id() & 32) ? p[0] : p[1]); }
  return __builtin_bit_cast(float, r);
}
__device__ __forceinline__ float wave_sum(float v) {
  v += sx<1>(v); v += sx<2>(v); v += sx<4>(v); v += sx<8>(v); v += sx<16>(v); v += sx<32>(v);
  return v;
}
__device__ __forceinline__ float wave_max(float v) {
  v = fmaxf(v, sx<1>(v)); v = fmaxf(v, sx<2>(v)); v = fmaxf(v, sx<4>(v)); v = fmaxf(v, sx<8>(v)); v = fmaxf(v, sx<16>(v)); v = fmaxf(v, sx<32>(v));
  return v;
}
__device__ __forceinline__ float ada_h(float x, float mb, float den, float cmod, float smod) {
  const float xn = rbf(rbf(x - mb) / den);
  return rbf(rbf(xn * rbf(1.0f + cmod)) + smod);
}
__device__ __forceinline__ void fma8(float xv, u32x4 w, float (&acc)[8]) {
#pragma unroll
  for (int k = 0; k < 4; ++k) {
    acc[2 * k] = fmaf(xv, lo_f(w[k]), acc[2 * k]);
    acc[2 * k + 1] = fmaf(xv, hi_f(w[k]), acc[2 * k + 1]);
  }
}
__device__ __forceinline__ float gelu_tanh(float v) {
  const float inner = 0.7978845608028654f * (v + 0.044715f * v * v * v);
  return 0.5f * v * (1.0f + tanhf(inner));
}

#define STAMP(slot) do { } while (0)
__global__ void __launch_bounds__(256) k_mod(const bf16s* __restrict__ c, const bf16s* __restrict__ wmod, float* __restrict__ ws) {
  __shared__ __attribute__((aligned(16))) float xs[128];
  __shared__ __attribute__((aligned(16))) float red[4 * 64];
  const int tid = threadIdx.x, lane = tid & 63, wave = tid >> 6;
  const int cg = blockIdx.x % 96, ks = blockIdx.x / 96;
  u32x4 w[4];
#pragma unroll
  for (int i = 0; i < 4; ++i) w[i] = ldnt(wmod + (size_t)(ks * 128 + (i * 4 + wave) * 8 + (lane >> 3)) * NMOD + cg * 64 + (lane & 7) * 8);
  {
    for (int idx = blockIdx.x * 256 + tid; idx < HO_VEC4; idx += 96 * SM * 256)
      *reinterpret_cast<u32x4*>(ws + WS_HO + (size_t)idx * 4) = u32x4{SENT, SENT, SENT, SENT};
  }
  if (tid < 128) { const float cv = bf2f(c[ks * 128 + tid]); xs[tid] = rbf(cv / (1.0f + expf(-cv))); }
  __syncthreads();
  float acc[8] = {0, 0, 0, 0, 0, 0, 0, 0};
#pragma unroll
  for (int i = 0; i < 4; ++i) fma8(xs[(i * 4 + wave) * 8 + (lane >> 3)], w[i], acc);
#pragma unroll
  for (int off = 8; off < 64; off <<= 1)
#pragma unroll
    for (int k = 0; k < 8; ++k) acc[k] += __shfl_xor(acc[k], off);
  if (lane < 8) {
    *reinterpret_cast<f32x4*>(red + wave * 64 + lane * 8) = f32x4{acc[0], acc[1], acc[2], acc[3]};
    *reinterpret_cast<f32x4*>(red + wave * 64 + lane * 8 + 4) = f32x4{acc[4], acc[5], acc[6], acc[7]};
  }
  __syncthreads();
  if (tid < 64) ws[WS_MODP + (size_t)ks * NMOD + cg * 64 + tid] = (red[tid] + red[64 + tid]) + (red[128 + tid] + red[192 + tid]);
}

constexpr int REC = 4 + DH;
__global__ void __launch_bounds__(512, 2) k_mega(const bf16s* __restrict__ x, const bf16s* __restrict__ kc, const bf16s* __restrict__ vc,
                                                 const bf16s* __restrict__ wq, const bf16s* __restrict__ wo, const bf16s* __restrict__ wg,
                                                 const bf16s* __restrict__ wu, const bf16s* __restrict__ wd, float* __restrict__ ws,
                                                 __hip_bfloat16* __restrict__ out) {
  __shared__ __attribute__((aligned(16))) bf16s qs[8 * DH];
  __shared__ __attribute__((aligned(16))) float mrg[8 * 8 * REC];
  __shared__ __attribute__((aligned(16))) float xs0[256];
  __shared__ __attribute__((aligned(16))) float osl[16];
  __shared__ __attribute__((aligned(16))) float xsb[256];
  __shared__ __attribute__((aligned(16))) float redb[4 * 128];
  __shared__ __attribute__((aligned(16))) float actl[16];
  __shared__ float x1l[4];
  __shared__ int flg[8];
  const int tid = threadIdx.x, lane = tid & 63, wave = __builtin_amdgcn_readfirstlane(tid >> 6);
  const int b = blockIdx.x;
  const int g = b >> 6, sp = b & 63;
  const int lg = lane >> 4, lc = lane & 15;
  const rsrc_t hr = __builtin_amdgcn_make_buffer_rsrc((void*)ws, 0, (int)(WS_END * 4), 0x00020000);

  const size_t key0 = (size_t)g * SK + (size_t)(sp * 8 + wave) * 16;
  const bf16s* kb = kc + (key0 + lc) * DH + lg * 8;
  const bf16s* vb = vc + (key0 + 4 * lg) * DH + lc * 8;
  u32x4 K[8][4], V[4][8];

  STAMP(0);
  if (tid < 8) flg[tid] = 0;
#define ISSUE_K() do { \
    _Pragma("unroll") for (int t = 0; t < 8; ++t) _Pragma("unroll") for (int ks = 0; ks < 4; ++ks) K[t][ks] = ldnt(kb + (size_t)t * 8192 * DH + ks * 32); \
  } while (0)
#define ISSUE_V(t) do { \
    _Pragma("unroll") for (int i = 0; i < 8; ++i) V[t][i] = ldnt(vb + (size_t)((t) * 16384 + (i >> 2) * 8192 + (i & 3)) * DH); \
  } while (0)
  if (wave != 0) {
    if (wave == 7 && lane < 16) {
      const int idx = 2 * DM + b * 16 + lane;
      float s = 0.f;
#pragma unroll
      for (int k = 0; k < SM; ++k) s += ws[WS_MODP + (size_t)k * NMOD + idx];
      ho_st1(hr, WS_MODF + idx, rbf(s));
    }
    wg_barrier();
    ISSUE_K();
    wg_barrier();
  } else {
    const int cg = b & 63, ks = b >> 6;
    __builtin_amdgcn_s_setprio(3);
    const u32x4 xa = *reinterpret_cast<const u32x4*>(x + lane * 16), xb = *reinterpret_cast<const u32x4*>(x + lane * 16 + 8);
    const u32x2 xr = *reinterpret_cast<const u32x2*>(x + ks * 256 + lane * 4);
    f32x4 sa = {0.f, 0.f, 0.f, 0.f}, ca = {0.f, 0.f, 0.f, 0.f};
#pragma unroll
    for (int s = 0; s < SM; ++s) {
      sa += *reinterpret_cast<const f32x4*>(ws + WS_MODP + (size_t)s * NMOD + ks * 256 + lane * 4);
      ca += *reinterpret_cast<const f32x4*>(ws + WS_MODP + (size_t)s * NMOD + DM + ks * 256 + lane * 4);
    }
    u32x4 w[32];
#pragma unroll
    for (int i = 0; i < 32; ++i) w[i] = ldnt(wq + (size_t)(ks * 256 + i * 8 + (lane >> 3)) * DQ + cg * 64 + (lane & 7) * 8);
    wg_barrier();
    float xv[16];
#pragma unroll
    for (int k = 0; k < 4; ++k) { xv[2 * k] = lo_f(xa[k]); xv[2 * k + 1] = hi_f(xa[k]); xv[8 + 2 * k] = lo_f(xb[k]); xv[9 + 2 * k] = hi_f(xb[k]); }
    float s = 0.f;
#pragma unroll
    for (int k = 0; k < 16; ++k) s += xv[k];
    const float mean = wave_sum(s) * (1.0f / DM);
    s = 0.f;
#pragma unroll
    for (int k = 0; k < 16; ++k) { const float d = xv[k] - mean; s = fmaf(d, d, s); }
    const float var = wave_sum(s) * (1.0f / DM);
    const float mb = rbf(mean), den = rbf(sqrtf(rbf(rbf(var) + 1e-5f)));
    f32x4 h4;
    h4[0] = ada_h(lo_f(xr[0]), mb, den, rbf(ca[0]), rbf(sa[0]));
    h4[1] = ada_h(hi_f(xr[0]), mb, den, rbf(ca[1]), rbf(sa[1]));
    h4[2] = ada_h(lo_f(xr[1]), mb, den, rbf(ca[2]), rbf(sa[2]));
    h4[3] = ada_h(hi_f(xr[1]), mb, den, rbf(ca[3]), rbf(sa[3]));
    *reinterpret_cast<f32x4*>(xs0 + lane * 4) = h4;
    lds_fence();
    float acc[8] = {0, 0, 0, 0, 0, 0, 0, 0};
#pragma unroll
    for (int i = 0; i < 32; ++i) fma8(xs0[i * 8 + (lane >> 3)], w[i], acc);
#pragma unroll
    for (int off = 8; off < 64; off <<= 1)
#pragma unroll
      for (int k = 0; k < 8; ++k) acc[k] += __shfl_xor(acc[k], off);
    if (lane < 8) {
      ho_st4(hr, WS_QP + (size_t)ks * DQ + cg * 64 + lane * 8, f32x4{acc[0], acc[1], acc[2], acc[3]});
      ho_st4(hr, WS_QP + (size_t)ks * DQ + cg * 64 + lane * 8 + 4, f32x4{acc[4], acc[5], acc[6], acc[7]});
    }
    STAMP(1);
    u32x4 qv[4][4];
    for (int it = 0; it < MAXSPIN; ++it) {
      asm volatile("" ::: "memory");
      bool ok = true;
#pragma unroll
      for (int j = 0; j < 4; ++j)
#pragma unroll
        for (int k4 = 0; k4 < SQ; ++k4) { qv[j][k4] = ho_ld4(hr, WS_QP + (size_t)k4 * DQ + g * 1024 + (j * 64 + lane) * 4); }
#pragma unroll
      for (int j = 0; j < 4; ++j)
#pragma unroll
        for (int k4 = 0; k4 < SQ; ++k4) ok = ok & ok4(qv[j][k4]);
      if (__all(ok)) break;
      __builtin_amdgcn_s_sleep(2);
    }
    STAMP(2);
#pragma unroll
    for (int j = 0; j < 4; ++j) {
      float q4[4];
#pragma unroll
      for (int e = 0; e < 4; ++e) q4[e] = (u2f(qv[j][0][e]) + u2f(qv[j][1][e])) + (u2f(qv[j][2][e]) + u2f(qv[j][3][e]));
      u32x2 pk = {cvtpk(q4[0], q4[1]), cvtpk(q4[2], q4[3])};
      *reinterpret_cast<u32x2*>(qs + (j * 64 + lane) * 4) = pk;
    }
    wg_barrier();
    ISSUE_K();
  }
  STAMP(3);

  bf16x8 qf[4];
#pragma unroll
  for (int ks = 0; ks < 4; ++ks) {
    u32x4 z = {0u, 0u, 0u, 0u};
    if (lc < 8) z = *reinterpret_cast<const u32x4*>(qs + lc * DH + ks * 32 + lg * 8);
    qf[ks] = __builtin_bit_cast(bf16x8, z);
  }
  ISSUE_V(0);
  ISSUE_V(1);
  float sc[8][4];
#pragma unroll
  for (int t = 0; t < 8; ++t) {
    f32x4 d = {0.f, 0.f, 0.f, 0.f};
#pragma unroll
    for (int ks = 0; ks < 4; ++ks) d = __builtin_amdgcn_mfma_f32_16x16x32_bf16(__builtin_bit_cast(bf16x8, K[t][ks]), qf[ks], d, 0, 0, 0);
#pragma unroll
    for (int r = 0; r < 4; ++r) sc[t][r] = rbf(rbf(d[r]) * 0.08838834764831845f);
    if (t == 3) ISSUE_V(2);
    if (t == 7) ISSUE_V(3);
  }
  STAMP(4);
  float m = sc[0][0];
#pragma unroll
  for (int t = 0; t < 8; ++t)
#pragma unroll
    for (int r = 0; r < 4; ++r) m = fmaxf(m, sc[t][r]);
  m = fmaxf(m, sx<16>(m));
  m = fmaxf(m, sx<32>(m));
  float l = 0.f;
  u32x4 P[4];
#pragma unroll
  for (int t = 0; t < 4; ++t) {
    float p[8];
#pragma unroll
    for (int j = 0; j < 8; ++j) { p[j] = __builtin_amdgcn_exp2f((sc[2 * t + (j >> 2)][j & 3] - m) * 1.4426950408889634f); l += p[j]; }
#pragma unroll
    for (int k = 0; k < 4; ++k) P[t][k] = cvtpk(p[2 * k], p[2 * k + 1]);
  }
  l += sx<16>(l);
  l += sx<32>(l);
  f32x4 acc[8];
#pragma unroll
  for (int d = 0; d < 8; ++d) acc[d] = f32x4{0.f, 0.f, 0.f, 0.f};
#pragma unroll
  for (int t = 0; t < 4; ++t) {
#pragma unroll
    for (int d = 0; d < 8; ++d) {
      u32x4 bb;
#pragma unroll
      for (int p = 0; p < 4; ++p)
        bb[p] = __builtin_amdgcn_perm(V[t][2 * p + 1][d >> 1], V[t][2 * p][d >> 1], (d & 1) ? 0x07060302u : 0x05040100u);
      acc[d] = __builtin_amdgcn_mfma_f32_16x16x32_bf16(__builtin_bit_cast(bf16x8, P[t]), __builtin_bit_cast(bf16x8, bb), acc[d], 0, 0, 0);
    }
  }
  if (lg < 2) {
#pragma unroll
    for (int r = 0; r < 4; ++r) {
      float* rec = mrg + (wave * 8 + 4 * lg + r) * REC + 4 + 8 * lc;
      *reinterpret_cast<f32x4*>(rec) = f32x4{acc[0][r], acc[1][r], acc[2][r], acc[3][r]};
      *reinterpret_cast<f32x4*>(rec + 4) = f32x4{acc[4][r], acc[5][r], acc[6][r], acc[7][r]};
    }
  }
  if (lane < 8) { mrg[(wave * 8 + lane) * REC] = m; mrg[(wave * 8 + lane) * REC + 1] = l; }
  STAMP(5);
  wg_barrier();
  STAMP(6);
  if (tid < 256) {
    const int h = tid >> 5, dq = tid & 31;
    float M = mrg[h * REC];
#pragma unroll
    for (int w = 1; w < 8; ++w) M = fmaxf(M, mrg[(w * 8 + h) * REC]);
    float L = 0.f; f32x4 o = {0.f, 0.f, 0.f, 0.f};
#pragma unroll
    for (int w = 0; w < 8; ++w) {
      const float* rec = mrg + (w * 8 + h) * REC;
      const float wt = __builtin_amdgcn_exp2f((rec[0] - M) * 1.4426950408889634f);
      L = fmaf(wt, rec[1], L);
      const f32x4 v = *reinterpret_cast<const f32x4*>(rec + 4 + dq * 4);
      o[0] = fmaf(wt, v[0], o[0]); o[1] = fmaf(wt, v[1], o[1]); o[2] = fmaf(wt, v[2], o[2]); o[3] = fmaf(wt, v[3], o[3]);
    }
    const size_t slot = (size_t)(g * 8 + h) * NSPLIT + sp;
    ho_st4(hr, WS_PO + slot * DH + dq * 4, o);
    if (dq == 0) ho_st2(hr, WS_PML + slot * 2, M, L);
  }
  volatile __attribute__((address_space(3))) int* vflg = (volatile __attribute__((address_space(3))) int*)flg;
  const bool ffn = b < NFW;
  const int cgf = b % 56, ksf = b / 56;
#define TR_STEP(v, n, X) _Pragma("unroll") for (int i_ = 0; i_ < (n) / 2; ++i_) { \
    const bool up_ = (lane & (X)) != 0; const float keep_ = up_ ? v[i_ + (n) / 2] : v[i_], give_ = up_ ? v[i_] : v[i_ + (n) / 2]; \
    v[i_] = keep_ + sx<X>(give_); }
#define LDS_SPIN(idx, val) do { for (int it_ = 0; it_ < (MAXSPIN << 4) && vflg[idx] < (val); ++it_) __builtin_amdgcn_s_sleep(1); asm volatile("" ::: "memory"); } while (0)
  if (wave < 4) __builtin_amdgcn_s_setprio(3);
  if (wave == 0) {
    const int hd = b >> 3, ds = b & 7;
    u32x2 ml; u32x4 pv[4];
    for (int it = 0; it < MAXSPIN; ++it) {
      asm volatile("" ::: "memory");
      ml = ho_ld2(hr, WS_PML + ((size_t)hd * NSPLIT + lane) * 2);
#pragma unroll
      for (int i = 0; i < 4; ++i) pv[i] = ho_ld4(hr, WS_PO + ((size_t)hd * NSPLIT + lane) * DH + ds * 16 + i * 4);
      bool ok = (ml[0] != SENT) & (ml[1] != SENT);
#pragma unroll
      for (int i = 0; i < 4; ++i) ok = ok & ok4(pv[i]);
      if (__all(ok)) break;
      __builtin_amdgcn_s_sleep(1);
    }
    STAMP(7);
    const float ms = u2f(ml[0]);
    const float M = wave_max(ms);
    const float wt = __builtin_amdgcn_exp2f((ms - M) * 1.4426950408889634f);
    const float L = wave_sum(wt * u2f(ml[1]));
    float v[16];
#pragma unroll
    for (int i = 0; i < 16; ++i) v[i] = wt * u2f(pv[i >> 2][i & 3]);
    TR_STEP(v, 16, 32) TR_STEP(v, 8, 16) TR_STEP(v, 4, 8) TR_STEP(v, 2, 4)
    v[0] += sx<2>(v[0]); v[0] += sx<1>(v[0]);
    if ((lane & 3) == 0) osl[lane >> 2] = rbf(v[0] / L);
    lds_fence();
    if (lane == 0) vflg[0] = 1;
    STAMP(8);
    if (ffn) {
      u32x4 gu = {0u, 0u, 0u, 0u};
      LDS_SPIN(2, 4);
      for (int it = 0; it < MAXSPIN; ++it) {
        asm volatile("" ::: "memory");
        if (lane < 32) gu = ho_ld4(hr, WS_GUP + (size_t)b * 128 + lane * 4);
        if (__all(ok4(gu))) break;
        __builtin_amdgcn_s_sleep(1);
      }
      STAMP(14);
      float t4[4];
#pragma unroll
      for (int e = 0; e < 4; ++e) { t4[e] = u2f(gu[e]); t4[e] += sx<8>(t4[e]); t4[e] += sx<16>(t4[e]); }
#pragma unroll
      for (int e = 0; e < 4; ++e) {
        const float us = sx<4>(t4[e]);
        if (lane < 4) actl[lane * 4 + e] = rbf(rbf(gelu_tanh(rbf(t4[e]))) * rbf(us));
      }
      lds_fence();
      if (lane == 0) vflg[4] = 1;
    }
  } else if (wave == 1) {
    u32x4 ap[4], mg;
    const u32x2 xr = *reinterpret_cast<const u32x2*>(x + b * 4);
    LDS_SPIN(5, 4);
    for (int it = 0; it < MAXSPIN; ++it) {
      asm volatile("" ::: "memory");
#pragma unroll
      for (int k = 0; k < 4; ++k) ap[k] = ho_ld4(hr, WS_ATP + ((size_t)b * 256 + k * 64 + lane) * 4);
      mg = ho_ld4(hr, WS_MODF + 2 * DM + b * 4);
      const bool ok = ok4(ap[0]) & ok4(ap[1]) & ok4(ap[2]) & ok4(ap[3]) & ok4(mg);
      if (__all(ok)) break;
      __builtin_amdgcn_s_sleep(1);
    }
    STAMP(9);
    float v[4];
#pragma unroll
    for (int e = 0; e < 4; ++e) v[e] = (u2f(ap[0][e]) + u2f(ap[1][e])) + (u2f(ap[2][e]) + u2f(ap[3][e]));
    TR_STEP(v, 4, 32) TR_STEP(v, 2, 16)
    v[0] += sx<8>(v[0]); v[0] += sx<4>(v[0]); v[0] += sx<2>(v[0]); v[0] += sx<1>(v[0]);
    const int e = lane >> 4;
    const unsigned xw = (e & 2) ? xr[1] : xr[0];
    const float xin = (e & 1) ? hi_f(xw) : lo_f(xw);
    const unsigned gw = (e & 2) ? ((e & 1) ? mg[3] : mg[2]) : ((e & 1) ? mg[1] : mg[0]);
    const float x1 = rbf(xin + rbf(u2f(gw) * rbf(v[0])));
    if ((lane & 15) == 0) { ho_st1(hr, WS_X1 + b * 4 + e, x1); x1l[e] = x1; }
    lds_fence();
    if (lane == 0) vflg[3] = 1;
  } else if (wave == 2) {
  } else if (wave == 3) {
    u32x4 dv[4], gbv;
    if (ffn) LDS_SPIN(6, 4); else LDS_SPIN(3, 1);
    for (int it = 0; it < MAXSPIN; ++it) {
      asm volatile("" ::: "memory");
#pragma unroll
      for (int k = 0; k < 4; ++k) {
        dv[k] = u32x4{0u, 0u, 0u, 0u};
        if (k * 64 + lane < NFW) dv[k] = ho_ld4(hr, WS_DNP + ((size_t)b * NFW + k * 64 + lane) * 4);
      }
      gbv = ho_ld4(hr, WS_MODF + 5 * DM + b * 4);
      const bool ok = ok4(dv[0]) & ok4(dv[1]) & ok4(dv[2]) & ok4(dv[3]) & ok4(gbv);
      if (__all(ok)) break;
      __builtin_amdgcn_s_sleep(1);
    }
    STAMP(12);
    float v[4];
#pragma unroll
    for (int e = 0; e < 4; ++e) v[e] = (u2f(dv[0][e]) + u2f(dv[1][e])) + (u2f(dv[2][e]) + u2f(dv[3][e]));
    TR_STEP(v, 4, 32) TR_STEP(v, 2, 16)
    v[0] += sx<8>(v[0]); v[0] += sx<4>(v[0]); v[0] += sx<2>(v[0]); v[0] += sx<1>(v[0]);
    LDS_SPIN(3, 1);
    const int e = lane >> 4;
    const unsigned gw = (e & 2) ? ((e & 1) ? gbv[3] : gbv[2]) : ((e & 1) ? gbv[1] : gbv[0]);
    if ((lane & 15) == 0) out[b * 4 + e] = __float2bfloat16(x1l[e] + rbf(u2f(gw) * rbf(v[0])));
    STAMP(13);
  } else {
    const int gw = wave - 4;
    u32x4 Wo[8], Wg[8], Wu[8], Wd[8];
    {
      const bf16s* p = wo + (size_t)(b * 16 + (lane >> 5)) * DM + gw * 256 + (lane & 31) * 8;
#pragma unroll
      for (int i = 0; i < 8; ++i) Wo[i] = ldnt(p + (size_t)i * 2 * DM);
    }
    LDS_SPIN(0, 1);
    {
      float a8[8] = {0, 0, 0, 0, 0, 0, 0, 0};
#pragma unroll
      for (int i = 0; i < 8; ++i) fma8(osl[2 * i + (lane >> 5)], Wo[i], a8);
#pragma unroll
      for (int k = 0; k < 8; ++k) a8[k] += sx<32>(a8[k]);
      if (lane < 32) {
        const size_t q0 = (size_t)gw * 64 + lane * 2;
        ho_st4(hr, WS_ATP + (q0 * 256 + b) * 4, f32x4{a8[0], a8[1], a8[2], a8[3]});
        ho_st4(hr, WS_ATP + ((q0 + 1) * 256 + b) * 4, f32x4{a8[4], a8[5], a8[6], a8[7]});
      }
      if (lane == 0) __hip_atomic_fetch_add(&flg[5], 1, __ATOMIC_RELAXED, __HIP_MEMORY_SCOPE_WORKGROUP);
    }
    if (ffn) {
      LDS_SPIN(3, 1);
      {
        const size_t off = (size_t)(ksf * 256 + gw * 64 + (lane >> 3)) * FF + cgf * 64 + (lane & 7) * 8;
#pragma unroll
        for (int i = 0; i < 8; ++i) { Wg[i] = ldnt(wg + off + (size_t)i * 8 * FF); Wu[i] = ldnt(wu + off + (size_t)i * 8 * FF); }
      }
      const int myrow = ksf * 256 + gw * 64 + lane;
      u32x4 xq[4]; unsigned xm, sbm, cbm;
      for (int it = 0; it < MAXSPIN; ++it) {
        asm volatile("" ::: "memory");
        bool ok = true;
#pragma unroll
        for (int k = 0; k < 4; ++k) { xq[k] = ho_ld4(hr, WS_X1 + (k * 64 + lane) * 4); ok = ok & ok4(xq[k]); }
        xm = ho_ld1(hr, WS_X1 + myrow); sbm = ho_ld1(hr, WS_MODF + 3 * DM + myrow); cbm = ho_ld1(hr, WS_MODF + 4 * DM + myrow);
        ok = ok & (xm != SENT) & (sbm != SENT) & (cbm != SENT);
        if (__all(ok)) break;
        __builtin_amdgcn_s_sleep(1);
      }
      STAMP(10);
      {
        const bf16s* p = wd + (size_t)(b * 16 + (lane >> 5)) * DM + gw * 256 + (lane & 31) * 8;
#pragma unroll
        for (int i = 0; i < 8; ++i) Wd[i] = ldnt(p + (size_t)i * 2 * DM);
      }
      {
        float s = 0.f;
#pragma unroll
        for (int k = 0; k < 4; ++k) s += (u2f(xq[k][0]) + u2f(xq[k][1])) + (u2f(xq[k][2]) + u2f(xq[k][3]));
        const float mean = wave_sum(s) * (1.0f / DM);
        s = 0.f;
#pragma unroll
        for (int k = 0; k < 4; ++k)
#pragma unroll
          for (int e = 0; e < 4; ++e) { const float d = u2f(xq[k][e]) - mean; s = fmaf(d, d, s); }
        const float var = wave_sum(s) * (1.0f / DM);
        const float mb = rbf(mean), den = rbf(sqrtf(rbf(rbf(var) + 1e-5f)));
        xsb[gw * 64 + lane] = ada_h(u2f(xm), mb, den, u2f(cbm), u2f(sbm));
        lds_fence();
      }
      float v[16];
#pragma unroll
      for (int k = 0; k < 16; ++k) v[k] = 0.f;
#pragma unroll
      for (int i = 0; i < 8; ++i) {
        const float hv = xsb[gw * 64 + i * 8 + (lane >> 3)];
#pragma unroll
        for (int k = 0; k < 4; ++k) {
          v[2 * k] = fmaf(hv, lo_f(Wg[i][k]), v[2 * k]); v[2 * k + 1] = fmaf(hv, hi_f(Wg[i][k]), v[2 * k + 1]);
          v[8 + 2 * k] = fmaf(hv, lo_f(Wu[i][k]), v[8 + 2 * k]); v[9 + 2 * k] = fmaf(hv, hi_f(Wu[i][k]), v[9 + 2 * k]);
        }
      }
      TR_STEP(v, 16, 32) TR_STEP(v, 8, 16) TR_STEP(v, 4, 8)
      {
        const int vid = (lane >> 3) * 2, col = (lane & 7) * 8 + (vid & 7);
        *reinterpret_cast<float2*>(redb + gw * 128 + (vid >> 3) * 64 + col) = make_float2(v[0], v[1]);
      }
      lds_fence();
      if (lane == 0) __hip_atomic_fetch_add(&flg[2], 1, __ATOMIC_RELAXED, __HIP_MEMORY_SCOPE_WORKGROUP);
      LDS_SPIN(2, 4);
      if (lane < 8) {
        const int idx = (lane >> 2) * 64 + gw * 16 + (lane & 3) * 4;
        const f32x4 t = (*reinterpret_cast<const f32x4*>(redb + idx) + *reinterpret_cast<const f32x4*>(redb + 128 + idx)) +
                        (*reinterpret_cast<const f32x4*>(redb + 256 + idx) + *reinterpret_cast<const f32x4*>(redb + 384 + idx));
        ho_st4(hr, WS_GUP + (size_t)(cgf * 4 + gw) * 128 + ksf * 32 + lane * 4, t);
      }
      STAMP(11);
      LDS_SPIN(4, 1);
      float a8[8] = {0, 0, 0, 0, 0, 0, 0, 0};
#pragma unroll
      for (int i = 0; i < 8; ++i) fma8(actl[2 * i + (lane >> 5)], Wd[i], a8);
#pragma unroll
      for (int k = 0; k < 8; ++k) a8[k] += sx<32>(a8[k]);
      if (lane < 32) {
        const size_t q0 = (size_t)gw * 64 + lane * 2;
        ho_st4(hr, WS_DNP + (q0 * NFW + b) * 4, f32x4{a8[0], a8[1], a8[2], a8[3]});
        ho_st4(hr, WS_DNP + ((q0 + 1) * NFW + b) * 4, f32x4{a8[4], a8[5], a8[6], a8[7]});
      }
      if (lane == 0) __hip_atomic_fetch_add(&flg[6], 1, __ATOMIC_RELAXED, __HIP_MEMORY_SCOPE_WORKGROUP);
    }
  }
}

extern "C" void kernel_launch(void* const* d_in, const int* in_sizes, int n_in,
                              void* d_out, int out_size, void* d_ws, size_t ws_size,
                              hipStream_t stream) {
  const bf16s* x = (const bf16s*)d_in[0];
  const bf16s* kc = (const bf16s*)d_in[1];
  const bf16s* vc = (const bf16s*)d_in[2];
  const bf16s* wq = (const bf16s*)d_in[3];
  const bf16s* wo = (const bf16s*)d_in[4];
  const bf16s* wg = (const bf16s*)d_in[6];
  const bf16s* wu = (const bf16s*)d_in[7];
  const bf16s* wd = (const bf16s*)d_in[8];
  const bf16s* c = (const bf16s*)d_in[9];
  const bf16s* wmod = (const bf16s*)d_in[10];
  float* ws = (float*)d_ws;
  k_mod<<<96 * SM, 256, 0, stream>>>(c, wmod, ws);
  k_mega<<<256, 512, 0, stream>>>(x, kc, vc, wq, wo, wg, wu, wd, ws, (__hip_bfloat16*)d_out);
}
```
